```python
import torch

def _softmax(x, axis=-1):
    return torch.softmax(x, dim=axis)
def _gelu(x):
    return torch.nn.functional.gelu(x, approximate="tanh")
def _silu(x):
    return torch.nn.functional.silu(x)
def _sigmoid(x):
    return torch.sigmoid(x)
def _layernorm(x, axis=-1, eps=1e-5):
    m = x.mean(dim=axis, keepdim=True)
    v = x.var(dim=axis, keepdim=True, unbiased=False)
    return (x - m) / torch.sqrt(v + eps)
def _scatter_add(tgt, idx, src, axis=0):
    return tgt.clone().index_add_(axis, idx, src)
def _segment_sum(src, seg, n):
    out = torch.zeros((n,) + src.shape[1:], dtype=src.dtype)
    return out.index_add_(0, seg, src)
def _pad(x, pw):
    flat = tuple(v for pair in reversed(pw) for v in pair)
    return torch.nn.functional.pad(x, flat)

kernel_name = 'interleave_layers__sliding_block_cached__dense_activation_silu__decode'

def setup_inputs():

    _w = (lambda a: (lambda t: t.to(torch.bfloat16) if t.is_floating_point() else t)(torch.as_tensor(a)))
    def _A_setup():
        B, Sq, D = 1, 1, 448
        Hq, Hkv, Sk, Dh = 16, 8, 65536, 256
        assert Hq % Hkv == 0, (Hq, Hkv)
        return {
            "x": _w((lambda *s: torch.randn(*s, dtype=torch.bfloat16) * 0.05)(B, Sq, D)),
            "k_cache": _w((lambda *s: torch.randn(*s, dtype=torch.bfloat16) * 0.8)(B, Hkv, Sk, Dh)),
            "v_cache": _w((lambda *s: torch.randn(*s, dtype=torch.bfloat16) * 1.6)(B, Hkv, Sk, Dh)),
            "wq": _w((lambda *s: torch.randn(*s, dtype=torch.bfloat16) * 0.8)(D, Hq * Dh) * (D ** -0.5)),
            "wo": _w((lambda *s: torch.randn(*s, dtype=torch.bfloat16) * 1.6)(Hq * Dh, D) * ((Hq * Dh) ** -0.5)),
        }
    _A_in = _A_setup()

    def _B_setup():
        B, S, D, F = 1, 1, 448, 1792
        return {
            "x": _w((lambda *s: torch.randn(*s, dtype=torch.bfloat16) * 0.05)(B, S, D)),
            "w1": _w((lambda *s: torch.randn(*s, dtype=torch.bfloat16) * 0.4)(D, F) * (D ** -0.5)),
            "w2": _w((lambda *s: torch.randn(*s, dtype=torch.bfloat16) * 0.4)(F, D) * (F ** -0.5)),
        }
    _B_in = _B_setup()

    return {
        "A_x": _A_in["x"],
        "A_k_cache": _A_in["k_cache"],
        "A_v_cache": _A_in["v_cache"],
        "A_wq": _A_in["wq"],
        "A_wo": _A_in["wo"],
        "B_x": _B_in["x"],
        "B_w1": _B_in["w1"],
        "B_w2": _B_in["w2"],
    }

def reference(A_x, A_k_cache, A_v_cache, A_wq, A_wo, B_x, B_w1, B_w2):
    def _A_ref(x, k_cache, v_cache, wq, wo):
        B, Sq, D = x.shape
        Hq, Dh = 16, 256
        Hkv, Sk = k_cache.shape[1], k_cache.shape[2]
        W = min(4096, max(256, Sk // 4))
        rep = Hq // Hkv
        q = (lambda x, *d: x.permute(*d))((x @ wq).reshape(B, Sq, Hq, Dh), 0, 2, 1, 3)
        k = (lambda x, n, axis: torch.repeat_interleave(x, n, dim=axis))(k_cache[:, :, Sk - W:, :], rep, axis=1)
        v = (lambda x, n, axis: torch.repeat_interleave(x, n, dim=axis))(v_cache[:, :, Sk - W:, :], rep, axis=1)
        scores = torch.einsum("bhqd,bhkd->bhqk", q, k) * (Dh ** -0.5)
        p = _softmax(scores, axis=-1)
        o = torch.einsum("bhqk,bhkd->bhqd", p, v)
        return (lambda x, *d: x.permute(*d))(o, 0, 2, 1, 3).reshape(B, Sq, Hq * Dh) @ wo

    def _B_ref(x, w1, w2):
        h = torch.einsum("bsd,df->bsf", x, w1)
        h = _silu(h)
        out = torch.einsum("bsf,fd->bsd", h, w2)
        return out

    x = A_x
    x = x + _A_ref(_layernorm(x, axis=-1), A_k_cache, A_v_cache, A_wq, A_wo)
    x = x + _B_ref(_layernorm(x, axis=-1), B_w1, B_w2)
    x = x + _A_ref(_layernorm(x, axis=-1), A_k_cache, A_v_cache, A_wq, A_wo)
    x = x + _B_ref(_layernorm(x, axis=-1), B_w1, B_w2)
    return x
```

```cpp
#include <hip/hip_runtime.h>
#include <hip/hip_bf16.h>
typedef unsigned short u16;
__device__ __forceinline__ float bf2f(u16 b){ return __builtin_bit_cast(float, ((unsigned)b)<<16); }
__device__ __forceinline__ float rbf(float f){ return __bfloat162float(__float2bfloat16(f)); }
constexpr int D=448, HQ=16, HKV=8, SK=65536, DH=256, W=4096, F=1792;

__device__ void layernorm(const float* x, float* xn_lds){
  __shared__ float red[8];
  int t=threadIdx.x;
  float a = x[t], b = (t+256<D)? x[t+256]:0.f;
  float s=a+b;
  for(int o=32;o;o>>=1) s+=__shfl_xor(s,o);
  if((t&63)==0) red[t>>6]=s;
  __syncthreads();
  float mean=(red[0]+red[1]+red[2]+red[3])/D;
  float da=a-mean, db=(t+256<D)? b-mean:0.f;
  float v=da*da+db*db;
  for(int o=32;o;o>>=1) v+=__shfl_xor(v,o);
  if((t&63)==0) red[4+(t>>6)]=v;
  __syncthreads();
  float var=(red[4]+red[5]+red[6]+red[7])/D;
  float mb=rbf(mean), vb=rbf(var);
  float sd=rbf(sqrtf(rbf(vb+1e-5f)));
  xn_lds[t]=rbf(rbf(a-mb)/sd);
  if(t+256<D) xn_lds[t+256]=rbf(rbf(b-mb)/sd);
  __syncthreads();
}
__global__ void k_init(const u16* x, float* xf){ int t=threadIdx.x; if(t<D) xf[t]=bf2f(x[t]); }
__global__ void k_q(const float* x, const u16* wq, float* q){
  __shared__ float xn[D];
  layernorm(x,xn);
  int c=blockIdx.x*256+threadIdx.x;
  float s=0; for(int d=0;d<D;d++) s+=xn[d]*bf2f(wq[(size_t)d*4096+c]);
  q[c]=rbf(s);
}
__global__ void k_attn(const float* q,const u16* K,const u16* V,float* po,float* pm,float* pl){
  int g=blockIdx.x&7, s=blockIdx.x>>3; int t=threadIdx.x;
  __shared__ float qs[512]; __shared__ float p[256]; __shared__ float red[8];
  qs[t]=q[g*512+t]; qs[t+256]=q[g*512+256+t];
  __syncthreads();
  int h=t>>7, k=t&127;
  const u16* kr=K+((size_t)g*SK+(SK-W)+s*128+k)*DH;
  float sc=0; for(int d=0;d<DH;d++) sc+=qs[h*256+d]*bf2f(kr[d]);
  sc=rbf(sc)*0.0625f;
  float m=sc; for(int o=32;o;o>>=1) m=fmaxf(m,__shfl_xor(m,o));
  if((t&63)==0) red[t>>6]=m;
  __syncthreads();
  m=fmaxf(red[h*2],red[h*2+1]);
  float e=__expf(sc-m);
  p[t]=e;
  float l=e; for(int o=32;o;o>>=1) l+=__shfl_xor(l,o);
  if((t&63)==0) red[4+(t>>6)]=l;
  __syncthreads();
  float o0=0,o1=0;
  const u16* vr=V+((size_t)g*SK+(SK-W)+s*128)*DH+t;
  for(int kk=0;kk<128;kk++){ float v=bf2f(vr[kk*DH]); o0+=p[kk]*v; o1+=p[128+kk]*v; }
  po[((size_t)(g*2+0)*32+s)*256+t]=o0;
  po[((size_t)(g*2+1)*32+s)*256+t]=o1;
  if(t==0){ pm[(g*2)*32+s]=fmaxf(red[0],red[1]); pm[(g*2+1)*32+s]=fmaxf(red[2],red[3]); pl[(g*2)*32+s]=red[4]+red[5]; pl[(g*2+1)*32+s]=red[6]+red[7]; }
}
__global__ void k_comb(const float* po,const float* pm,const float* pl,float* o){
  int h=blockIdx.x,t=threadIdx.x;
  float M=-1e30f; for(int s=0;s<32;s++) M=fmaxf(M,pm[h*32+s]);
  float L=0,acc=0; for(int s=0;s<32;s++){ float w=__expf(pm[h*32+s]-M); L+=w*pl[h*32+s]; acc+=w*po[((size_t)h*32+s)*256+t]; }
  o[h*256+t]=rbf(acc/L);
}
__global__ void k_oproj(const float* o,const u16* wo,const float* x,float* x1){
  int c=blockIdx.x,t=threadIdx.x; __shared__ float red[4];
  float s=0; for(int r=t;r<4096;r+=256) s+=o[r]*bf2f(wo[(size_t)r*D+c]);
  for(int of=32;of;of>>=1) s+=__shfl_xor(s,of);
  if((t&63)==0) red[t>>6]=s; __syncthreads();
  if(t==0) x1[c]=rbf(x[c]+rbf(red[0]+red[1]+red[2]+red[3]));
}
__global__ void k_f1(const float* x,const u16* w1,float* h){
  __shared__ float xn[D];
  layernorm(x,xn);
  int c=blockIdx.x*256+threadIdx.x;
  float s=0; for(int d=0;d<D;d++) s+=xn[d]*bf2f(w1[(size_t)d*F+c]);
  s=rbf(s); h[c]=rbf(s/(1.f+__expf(-s)));
}
__global__ void k_f2(const float* h,const u16* w2,const float* x,float* x2,u16* out){
  int c=blockIdx.x,t=threadIdx.x; __shared__ float red[4];
  float s=0; for(int r=t;r<F;r+=256) s+=h[r]*bf2f(w2[(size_t)r*D+c]);
  for(int of=32;of;of>>=1) s+=__shfl_xor(s,of);
  if((t&63)==0) red[t>>6]=s; __syncthreads();
  if(t==0){ float v=rbf(x[c]+rbf(red[0]+red[1]+red[2]+red[3])); x2[c]=v; if(out) out[c]=(u16)(__builtin_bit_cast(unsigned,v)>>16);}
}
extern "C" void kernel_launch(void* const* d_in, const int* in_sizes, int n_in, void* d_out, int out_size, void* d_ws, size_t ws_size, hipStream_t stream){
  const u16* x=(const u16*)d_in[0]; const u16* K=(const u16*)d_in[1]; const u16* V=(const u16*)d_in[2];
  const u16* wq=(const u16*)d_in[3]; const u16* wo=(const u16*)d_in[4]; const u16* w1=(const u16*)d_in[6]; const u16* w2=(const u16*)d_in[7];
  float* ws=(float*)d_ws; float* xa=ws; float* xb=ws+512; float* q=ws+1024; float* o=q+4096; float* h=o+4096; float* pm=h+2048; float* pl=pm+512; float* po=pl+512;
  k_init<<<1,512,0,stream>>>(x,xa);
  for(int l=0;l<2;l++){
    k_q<<<16,256,0,stream>>>(xa,wq,q);
    k_attn<<<256,256,0,stream>>>(q,K,V,po,pm,pl);
    k_comb<<<16,256,0,stream>>>(po,pm,pl,o);
    k_oproj<<<448,256,0,stream>>>(o,wo,xa,xb);
    k_f1<<<7,256,0,stream>>>(xb,w1,h);
    k_f2<<<448,256,0,stream>>>(h,w2,xb,xa,l==1?(u16*)d_out:nullptr);
  }
}
```

```cpp
#include <hip/hip_runtime.h>
#include <hip/hip_bf16.h>

typedef unsigned u32;
typedef unsigned long long u64;
typedef u32 v4u __attribute__((ext_vector_type(4)));
typedef u32 v2u __attribute__((ext_vector_type(2)));
typedef float v4f __attribute__((ext_vector_type(4)));
typedef float v2f __attribute__((ext_vector_type(2)));
typedef short v4s __attribute__((ext_vector_type(4)));
typedef __bf16 v8bf __attribute__((ext_vector_type(8)));
typedef __bf16 v2bf __attribute__((ext_vector_type(2)));
#define LAS __attribute__((address_space(3)))
typedef __amdgpu_buffer_rsrc_t rsrc_t;

namespace {
constexpr int kSK = 65536, kW = 4096;
constexpr u32 WS_Q = 4096;
constexpr u32 WS_A = WS_Q + 2 * 8 * 32 * 128;
constexpr u32 WS_S = WS_A + 2 * 8 * 32768;
constexpr u32 WS_B = WS_S + 2 * 8 * 8192;
constexpr u32 WS_C = WS_B + 2 * 7 * 2048;
constexpr u32 WS_END = WS_C + 2 * 7 * 4096;
constexpr u32 OOB = 0x80000000u;
constexpr u32 L_QS = 0;
constexpr u32 L_OP = L_QS + 1280;
constexpr u32 L_ST = L_OP + 8192;
constexpr u32 L_XS = L_ST + 64;
constexpr u32 L_HS = L_XS + 896;
constexpr u32 L_RED = L_HS + 896;
constexpr u32 L_LN = L_RED + 2048;
constexpr u32 L_CS = L_LN + 64;
constexpr u32 L_WQ = 16384;
static_assert(L_CS + 1792 <= L_WQ, "lds");
constexpr u32 L_K = 32768, L_V = L_K + 65536, L_END = L_V + 65536;
static_assert(L_END <= 163840, "lds");
constexpr int SC1 = 16;
constexpr u32 SENT = 0xffffffffu;
constexpr int kSpin = 1 << 16;

#define D_Q0 70
#define D_Q1 4
#define D_A 4
#define D_B 30
#define D_C 40
#define D_RT 2
#define D_TAIL 5

__device__ __forceinline__ rsrc_t mk(const void* p, u32 n) { return __builtin_amdgcn_make_buffer_rsrc((void*)p, 0, n, 0x00020000); }
__device__ __forceinline__ void dma_nt(u32 lds, rsrc_t r, u32 voff, u32 soff) {
  asm volatile("s_mov_b32 m0, %0\n\ts_nop 0\n\tbuffer_load_dwordx4 %1, %2, %3 offen nt lds" ::"s"(lds), "v"(voff), "s"(r), "s"(soff) : "memory");
}
__device__ __forceinline__ void dma_sc1(u32 lds, rsrc_t r, u32 voff, u32 soff) {
  asm volatile("s_mov_b32 m0, %0\n\ts_nop 0\n\tbuffer_load_dwordx4 %1, %2, %3 offen sc1 lds" ::"s"(lds), "v"(voff), "s"(r), "s"(soff) : "memory");
}
#define WAITV(n) asm volatile("s_waitcnt vmcnt(" #n ")" ::: "memory")
#define BAR() asm volatile("s_waitcnt lgkmcnt(0)\n\ts_barrier" ::: "memory")
#define WAITL() asm volatile("s_waitcnt lgkmcnt(0)" ::: "memory")
template <int N> __device__ __forceinline__ void nap() {
  if constexpr (N > 127) { __builtin_amdgcn_s_sleep(127); nap<N - 127>(); }
  else if constexpr (N > 0) __builtin_amdgcn_s_sleep(N);
}
__device__ __forceinline__ float asf(u32 x) { return __builtin_bit_cast(float, x); }
__device__ __forceinline__ u32 asu(float x) { return __builtin_bit_cast(u32, x); }
__device__ __forceinline__ v4f mfma(v4u a, v4u b, v4f c) {
  return __builtin_amdgcn_mfma_f32_16x16x32_bf16(__builtin_bit_cast(v8bf, a), __builtin_bit_cast(v8bf, b), c, 0, 0, 0);
}
__device__ __forceinline__ u32 pk(float a, float b) { v2bf c; c.x = (__bf16)a; c.y = (__bf16)b; return __builtin_bit_cast(u32, c); }
__device__ __forceinline__ float rbf(float a) { return asf(pk(a, a) << 16); }
template <typename T> __device__ __forceinline__ T lds(u32 a) { return *(const LAS T*)a; }
template <typename T> __device__ __forceinline__ void sts(u32 a, T v) { *(LAS T*)a = v; }
__device__ __forceinline__ v2u trd(u32 a) { return __builtin_bit_cast(v2u, __builtin_amdgcn_ds_read_tr16_b64_v4i16((LAS v4s*)a)); }
__device__ __forceinline__ v4u tr2(u32 a0, u32 a1) { v2u x = trd(a0), y = trd(a1); return v4u{x.x, x.y, y.x, y.y}; }
template <int C> __device__ __forceinline__ float dpp(float x) { return asf(__builtin_amdgcn_update_dpp(0, asu(x), C, 0xf, 0xf, true)); }
constexpr int ROR8 = 0x128, ROR4 = 0x124, ROR2 = 0x122, ROR1 = 0x121, QX1 = 0xb1, QX2 = 0x4e;
__device__ __forceinline__ float x16(float x, bool mx) { auto r = __builtin_amdgcn_permlane16_swap(asu(x), asu(x), false, false); return mx ? fmaxf(asf(r[0]), asf(r[1])) : asf(r[0]) + asf(r[1]); }
__device__ __forceinline__ float x32(float x, bool mx) { auto r = __builtin_amdgcn_permlane32_swap(asu(x), asu(x), false, false); return mx ? fmaxf(asf(r[0]), asf(r[1])) : asf(r[0]) + asf(r[1]); }
__device__ __forceinline__ float s16(float a, float b) { auto r = __builtin_amdgcn_permlane16_swap(asu(a), asu(b), false, false); return asf(r[0]) + asf(r[1]); }
__device__ __forceinline__ float s32(float a, float b) { auto r = __builtin_amdgcn_permlane32_swap(asu(a), asu(b), false, false); return asf(r[0]) + asf(r[1]); }
__device__ __forceinline__ float wsum(float x) {
  x += dpp<ROR8>(x); x += dpp<ROR4>(x); x += dpp<ROR2>(x); x += dpp<ROR1>(x);
  return x32(x16(x, false), false);
}
__device__ __forceinline__ u32 mx4(v4u a) { return max(max(a.x, a.y), max(a.z, a.w)); }
__device__ __forceinline__ void acc8(float (&s)[8], v4u r) {
#pragma unroll
  for (int j = 0; j < 4; ++j) { s[2 * j] += asf(r[j] << 16); s[2 * j + 1] += asf(r[j] & 0xffff0000u); }
}
__device__ __forceinline__ float rscat(const float (&s)[8], int lane) {
  float t0 = s32(s[0], s[4]), t1 = s32(s[1], s[5]), t2 = s32(s[2], s[6]), t3 = s32(s[3], s[7]);
  float u0 = s16(t0, t2), u1 = s16(t1, t3);
  const bool b3 = lane & 8;
  return (b3 ? u1 : u0) + dpp<ROR8>(b3 ? u0 : u1);
}
__device__ __forceinline__ void ln_part(float x, int w, int lane) {
  const float x0 = asf(__builtin_amdgcn_readfirstlane(asu(x)));
  const float d = x - x0;
  const float s1 = wsum(d), s2 = wsum(d * d);
  if (lane == 0) sts<v2f>(L_LN + w * 8, v2f{x0 + s1 * (1.f / 64), s2 - s1 * s1 * (1.f / 64)});
}
__device__ __forceinline__ void ln_fin(float& mean, float& rstd) {
  const v4f a = lds<v4f>(L_LN), b = lds<v4f>(L_LN + 16), c = lds<v4f>(L_LN + 32);
  const v2f d = lds<v2f>(L_LN + 48);
  mean = (((a.x + a.z) + (b.x + b.z)) + ((c.x + c.z) + d.x)) * (1.f / 7);
  const float e0 = a.x - mean, e1 = a.z - mean, e2 = b.x - mean, e3 = b.z - mean, e4 = c.x - mean, e5 = c.z - mean, e6 = d.x - mean;
  const float m2 = (((a.y + a.w) + (b.y + b.w)) + ((c.y + c.w) + d.y)) + 64.f * ((e0 * e0 + e1 * e1 + e2 * e2 + e3 * e3) + (e4 * e4 + e5 * e5 + e6 * e6));
  rstd = __builtin_amdgcn_rsqf(m2 * (1.f / 448) + 1e-5f);
}
__device__ __forceinline__ float pick(float a, float b, float c, float d, int q) {
  const float lo = (q & 1) ? b : a, hi = (q & 1) ? d : c;
  return (q & 2) ? hi : lo;
}
__device__ __forceinline__ u32 wtr(u32 base, int lane) {
  const int G = lane >> 4, r = (lane >> 2) & 3, p = lane & 3;
  return base + (8 * G + r) * 128 + ((((r >> 1) & 1) | ((G & 1) << 1)) << 5) + p * 8;
}
__device__ __forceinline__ v4u wfrag(u32 a, int ks, int ct) { const u32 b = (a ^ (ct << 5)) + ks * 4096; return tr2(b, b + 512); }
__device__ __forceinline__ u32 wsrc(int lane, int odd) { return (u32)(((lane & 7) ^ ((((lane >> 4) & 1) | (odd << 1)) << 1)) << 4); }

#define TS(i)
#define TSL(i, j)
#define TSOUT()
#define NPRE 4
#define LD16(o) __builtin_amdgcn_raw_buffer_load_b128(rw, (o), 0, SC1)

__device__ __forceinline__ float gather_c(rsrc_t rw, u32 base, int lane) {
  const u32 o = base + lane * 16, o3 = lane < 40 ? o + 3072 : OOB;
  v4u r0 = LD16(o), r1 = LD16(o + 1024), r2 = LD16(o + 2048), r3 = LD16(o3);
  for (int sp = 0; sp < kSpin; ++sp) {
    const bool bad = max(max(mx4(r0), mx4(r1)), max(mx4(r2), mx4(r3))) == SENT;
    if (!__any(bad)) break;
    nap<D_RT>();
    asm volatile("" ::: "memory");
    if (bad) { r0 = LD16(o); r1 = LD16(o + 1024); r2 = LD16(o + 2048); r3 = LD16(o3); }
  }
  float s[8] = {0.f, 0.f, 0.f, 0.f, 0.f, 0.f, 0.f, 0.f};
  acc8(s, r0); acc8(s, r1); acc8(s, r2); acc8(s, r3);
  return rscat(s, lane);
}

__global__ void __launch_bounds__(768, 3)
fused_kernel(const unsigned short* __restrict__ xin, const char* __restrict__ Kc, const char* __restrict__ Vc, const char* __restrict__ wq,
             const char* __restrict__ wo, const char* __restrict__ w1, const char* __restrict__ w2, unsigned short* __restrict__ out,
             char* __restrict__ ws) {
  TS(0);
  const int lane = threadIdx.x & 63, w = __builtin_amdgcn_readfirstlane(threadIdx.x >> 6);
  const int g = blockIdx.x & 7, m = blockIdx.x >> 3;
  const int n = lane & 15, q = lane >> 4;
  const bool is_o = m < 14, is_f = m >= 28 && (m - 28) * 8 + g < 28;
  const rsrc_t rw = mk(ws, WS_END);
  const v4f Z = {0.f, 0.f, 0.f, 0.f};

  if (w < 4) {
    const int a = w;
    v4u kf[16], vf[16];
    {
      const size_t cu = ((size_t)g * kSK + (kSK - kW) + m * 128) * 512;
      const rsrc_t rk = mk(Kc + cu, 65536), rv = mk(Vc + cu, 65536);
      const int hi = lane >> 5, p = lane & 31;
      const u32 vk = hi * 512 + ((p ^ hi) << 4), vv = hi * 512 + ((p ^ (hi << 1)) << 4);
#pragma unroll
      for (int i = 0; i < 16; ++i) dma_nt(L_K + a * 16384 + i * 1024, rk, vk ^ ((2 * i & 15) << 4), (((i + m) & 15) * 4 + a) * 1024);
#pragma unroll
      for (int i = 0; i < 16; ++i) dma_nt(L_V + a * 16384 + i * 1024, rv, vv ^ ((2 * i & 7) << 5), (((i + m) & 15) * 4 + a) * 1024);
      TS(1);
      WAITV(16);
      const u32 ka = L_K + a * 16384 + n * 512 + ((q ^ n) << 4);
#pragma unroll
      for (int i = 0; i < 16; ++i) kf[i] = lds<v4u>((ka ^ ((i & 3) << 6)) + ((i >> 2) & 1) * 256 + (i >> 3) * 8192);
      TS(2);
    }
#pragma unroll
    for (int L = 0; L < 2; ++L) {
      BAR();
      TSL(5, 15);
      v4f sc0 = Z, sc1 = Z;
#pragma unroll
      for (int i = 0; i < 8; ++i) {
        const v4u b = lds<v4u>(L_QS + (n & 1) * 576 + i * 64 + q * 16);
        sc0 = mfma(kf[i], b, sc0);
        sc1 = mfma(kf[8 + i], b, sc1);
      }
      constexpr float c = 1.4426950408889634f / 16;
      float mx = fmaxf(fmaxf(fmaxf(sc0.x, sc0.y), fmaxf(sc0.z, sc0.w)), fmaxf(fmaxf(sc1.x, sc1.y), fmaxf(sc1.z, sc1.w)));
      mx = x32(x16(mx, true), true) * c;
      v4f p0, p1;
#pragma unroll
      for (int j = 0; j < 4; ++j) { p0[j] = __builtin_amdgcn_exp2f(sc0[j] * c - mx); p1[j] = __builtin_amdgcn_exp2f(sc1[j] * c - mx); }
      const v4u P = {pk(p0.x, p0.y), pk(p0.z, p0.w), pk(p1.x, p1.y), pk(p1.z, p1.w)};
      const float l = x32(x16(((p0.x + p0.y) + (p0.z + p0.w)) + ((p1.x + p1.y) + (p1.z + p1.w)), false), false);
      if (lane < 2) { sts<float>(L_ST + lane * 32 + a * 4, mx); sts<float>(L_ST + lane * 32 + 16 + a * 4, l); }
      if (L == 0) {
        const int r = (lane >> 2) & 3, p = lane & 3;
        const u32 va = L_V + a * 16384 + (4 * q + r) * 512 + ((4 * (q & 1) + r) << 5) + p * 8;
        TS(6);
        WAITV(8);
#pragma unroll
        for (int i = 0; i < 16; ++i) { const v2u x = trd((va ^ ((i & 7) << 5)) + (i >> 3) * 256); vf[i].x = x.x; vf[i].y = x.y; }
        WAITV(0);
        TS(7);
#pragma unroll
        for (int t = 0; t < 8; ++t)
#pragma unroll
          for (int T = 0; T < 2; ++T) { const v2u x = trd((va ^ (t << 5)) + T * 256 + 8192); vf[8 * T + t].z = x.x; vf[8 * T + t].w = x.y; }
      }
      v4f o0 = Z, o1 = Z;
#pragma unroll
      for (int t = 0; t < 8; ++t) {
        const bool on = (n >> 1) == t;
        const v4u A = {on ? P.x : 0u, on ? P.y : 0u, on ? P.z : 0u, on ? P.w : 0u};
        o0 = mfma(A, vf[t], o0);
        o1 = mfma(A, vf[8 + t], o1);
      }
      const u32 oa = L_OP + a * 2048 + (32 * q + n) * 4;
#pragma unroll
      for (int r = 0; r < 4; ++r) {
        sts<float>(oa + (r & 1) * 1024 + (r >> 1) * 64, o0[r]);
        sts<float>(oa + (r & 1) * 1024 + (r >> 1) * 64 + 512, o1[r]);
      }
      BAR();
      TSL(8, 16);
      {
        const int h = a >> 1, e = 64 * (a & 1) + lane;
        const v4f ms = lds<v4f>(L_ST + h * 32), ls = lds<v4f>(L_ST + h * 32 + 16);
        v2f ov[4];
#pragma unroll
        for (int j = 0; j < 4; ++j) ov[j] = lds<v2f>(L_OP + j * 2048 + h * 1024 + e * 8);
        const float M = fmaxf(fmaxf(ms.x, ms.y), fmaxf(ms.z, ms.w));
        v4f fa;
#pragma unroll
        for (int j = 0; j < 4; ++j) fa[j] = __builtin_amdgcn_exp2f(ms[j] - M);
        const float Ls = (fa.x * ls.x + fa.y * ls.y) + (fa.z * ls.z + fa.w * ls.w);
        const float inv = __builtin_amdgcn_rcpf(Ls);
        const float y0 = ((fa.x * ov[0].x + fa.y * ov[1].x) + (fa.z * ov[2].x + fa.w * ov[3].x)) * inv;
        const float y1 = ((fa.x * ov[0].y + fa.y * ov[1].y) + (fa.z * ov[2].y + fa.w * ov[3].y)) * inv;
        __builtin_amdgcn_raw_buffer_store_b32(min(pk(y0, y1), SENT - 1), rw,
            WS_A + h * 16384 + (e >> 5) * 4096 + m * 128 + (((((e >> 2) & 7) ^ ((m >> 1) & 7)) << 4) | ((e & 3) << 2)), (L * 8 + g) * 32768, SC1);
        if (!(a & 1))
          __builtin_amdgcn_raw_buffer_store_b32(min(asu(M + __builtin_amdgcn_logf(Ls)), SENT - 1), rw,
              lane < 32 ? WS_S + h * 4096 + m * 128 + lane * 4 : OOB, (L * 8 + g) * 8192, SC1);
      }
      TSL(9, 17);
      if (L) break;
      if (is_o || is_f) { BAR(); BAR(); }
      BAR();
    }
    TSOUT();
    return;
  }

  const int v = w - 4;
  const int f = (m - 28) * 8 + g, hh = m & 1, cb = m >> 1;
  const v4u ONES = {0x3f803f80u, 0x3f803f80u, 0x3f803f80u, 0x3f803f80u};
  v4u wf[16];
  v4u qf2[2];
  float csq = 0.f, cs1 = 0.f, xres = 0.f;
  __builtin_amdgcn_s_setprio(3);
  const u32 tag = *(const u32*)ws * 0x9e3779b5u + 0x7f4a7c15u;
  if (v == 0) {
    const v4u S = {SENT, SENT, SENT, SENT};
    const u32 l8 = lane >> 3, c16 = (lane & 7) * 16;
    __builtin_amdgcn_raw_buffer_store_b128(S, rw, WS_A + l8 * 4096 + m * 128 + c16, g * 32768, SC1);
    __builtin_amdgcn_raw_buffer_store_b128(S, rw, WS_A + l8 * 4096 + m * 128 + c16, (8 + g) * 32768, SC1);
    u32 o3 = OOB;
    if (l8 < 4) o3 = WS_S + ((l8 >> 1) * 8 + g) * 8192 + (l8 & 1) * 4096 + m * 128 + c16;
    else if (l8 < 6 && is_o) o3 = WS_B + ((l8 & 1) * 7 + cb) * 2048 + (2 * g + hh) * 128 + c16;
    __builtin_amdgcn_raw_buffer_store_b128(S, rw, o3, 0, SC1);
    if (is_f) {
      const u32 o4 = l8 < 7 ? WS_C + l8 * 4096 + f * 128 + c16 : OOB;
      __builtin_amdgcn_raw_buffer_store_b128(S, rw, o4, 0, SC1);
      __builtin_amdgcn_raw_buffer_store_b128(S, rw, o4, 7 * 4096, SC1);
      if (f == 0) {
        __builtin_amdgcn_raw_buffer_store_b128(S, rw, o4, 28 * 128, SC1);
        __builtin_amdgcn_raw_buffer_store_b128(S, rw, o4, 7 * 4096 + 28 * 128, SC1);
      }
    }
    v4u xv;
    asm volatile("buffer_load_dwordx4 %0, %1, %2, 0 offen" : "=v"(xv) : "v"(lane * 16), "s"(mk(xin, 896)) : "memory");
    const rsrc_t rq = mk(wq, 448 * 8192);
    const u32 vq = (lane >> 1) * 8192 + (g * 512 + m * 16) * 2 + (lane & 1) * 16;
#pragma unroll
    for (int i = 0; i < 14; ++i) dma_nt(L_WQ + i * 1024, rq, vq, i * 32 * 8192);
    TS(1);
    asm volatile("s_waitcnt vmcnt(14)" : "+v"(xv)::"memory");
    float xs[8] = {0.f, 0.f, 0.f, 0.f, 0.f, 0.f, 0.f, 0.f};
    acc8(xs, xv);
    if (lane < 56) sts<v4u>(L_XS + lane * 16, xv);
    const float mean = wsum(((xs[0] + xs[1]) + (xs[2] + xs[3])) + ((xs[4] + xs[5]) + (xs[6] + xs[7]))) * (1.f / 448);
    float m2 = 0.f;
#pragma unroll
    for (int j = 0; j < 8; ++j) m2 += (xs[j] - mean) * (xs[j] - mean);
    const float rstd = __builtin_amdgcn_rsqf(wsum(lane < 56 ? m2 : 0.f) * (1.f / 448) + 1e-5f);
    WAITV(0);
    TS(2);
    v4f aq = Z, as = Z;
    const u32 qa = L_WQ + (8 * q + ((lane >> 2) & 3)) * 32 + (lane & 3) * 8;
#pragma unroll
    for (int i = 0; i < 14; ++i) {
      const v4u b = tr2(qa + i * 1024, qa + i * 1024 + 128);
      aq = mfma(lds<v4u>(L_XS + i * 64 + q * 16), b, aq);
      as = mfma(ONES, b, as);
    }
    csq = as.x;
    const v2u gq = {asu(rbf((aq.x - mean * csq) * rstd)), tag};
    __builtin_amdgcn_raw_buffer_store_b64(gq, rw, lane < 16 ? WS_Q + (g * 32 + m) * 128 + lane * 8 : OOB, 0, SC1);
    TS(3);
    nap<D_Q0 / 3>();
  } else if (v < 4) {
    nap<D_Q0>();
  }

#pragma unroll
  for (int L = 0; L < 2; ++L) {
    if (v < 4) {
      const u32 o = WS_Q + (L * 8 + g) * 4096 + v * 1024 + lane * 16, of = L ? o : WS_Q + (v * 64 + lane) * 128;
      v4u r = LD16(o), fl = r;
      if (L == 0) fl = LD16(of);
      for (int sp = 0; sp < kSpin; ++sp) {
        const bool bad = r.y != tag || r.w != tag || fl.y != tag || fl.w != tag;
        if (!__any(bad)) break;
        nap<D_RT>();
        asm volatile("" ::: "memory");
        if (bad) { r = LD16(o); fl = r; if (L == 0) fl = LD16(of); }
      }
      sts<u32>(L_QS + (v >> 1) * 576 + (v & 1) * 256 + lane * 4, (r.x >> 16) | (r.z & 0xffff0000u));
    }
    TSL(4, 14);
    BAR();
    TSL(5, 15);
    if (L == 0) {
      if (blockIdx.x == 0 && v == 1) __builtin_amdgcn_raw_buffer_store_b32(tag, rw, lane == 0 ? 0 : OOB, 0, SC1);
      __builtin_amdgcn_s_setprio(0);
      if (is_o) {
        const rsrc_t ro = mk(wo, 4096 * 896);
#pragma unroll
        for (int i = 0; i < 4; ++i)
          dma_nt(L_K + (v * 4 + i) * 1024, ro, (lane >> 3) * 896 + wsrc(lane, i & 1), ((2 * g + hh) * 256 + (v * 4 + i) * 8) * 896 + cb * 128);
      } else if (is_f && v < 7) {
        const rsrc_t r1 = mk(w1, 448 * 3584);
#pragma unroll
        for (int i = 0; i < NPRE; ++i)
          dma_nt(L_K + (v * 8 + i) * 1024, r1, (lane >> 3) * 3584 + wsrc(lane, i & 1), (v * 8 + i) * 8 * 3584 + f * 128);
      }
      if (v < 7) {
        const u32 qa = L_WQ + v * 2048 + (8 * q + ((lane >> 2) & 3)) * 32 + (lane & 3) * 8;
        qf2[0] = tr2(qa, qa + 128);
        qf2[1] = tr2(qa + 1024, qa + 1024 + 128);
      }
    }
    BAR();
    TSL(8, 16);
    if (is_o) {
      const int kp = v >> 1, st = v & 1;
      const u32 sa = WS_A + (L * 8 + g) * 32768 + hh * 16384 + kp * 4096 + st * 2048;
      const u32 so = WS_S + (L * 8 + g) * 8192 + hh * 4096 + (16 * st + 4 * q + (lane & 3)) * 128;
      const u32 stg = L_V + v * 2048, aa = stg + n * 128 + ((q ^ (n >> 1)) << 4);
      if (L == 0) {
        WAITV(0);
        BAR();
        const u32 wa = wtr(L_K + kp * 8192, lane);
#pragma unroll
        for (int i = 0; i < 8; ++i) wf[i] = wfrag(wa, i >> 2, i & 3);
      }
      nap<D_A>();
      v4u a0, a1;
      u32 lse;
      for (int sp = 0; sp < kSpin; ++sp) {
        dma_sc1(stg, rw, lane * 16, sa);
        dma_sc1(stg + 1024, rw, lane * 16, sa + 1024);
        lse = __builtin_amdgcn_raw_buffer_load_b32(rw, so, 0, SC1);
        WAITV(0);
        a0 = lds<v4u>(aa); a1 = lds<v4u>(aa ^ 64);
        if (!__any(max(max(mx4(a0), mx4(a1)), lse) == SENT)) break;
        nap<D_RT>();
      }
      TSL(10, 18);
      v4f y[4];
#pragma unroll
      for (int c = 0; c < 4; ++c) y[c] = mfma(a1, wf[4 + c], mfma(a0, wf[c], Z));
      float M = asf(lse);
      M = fmaxf(M, dpp<QX1>(M)); M = fmaxf(M, dpp<QX2>(M));
      M = x32(x16(M, true), true);
      const float e = __builtin_amdgcn_exp2f(asf(lse) - M);
      float Ls = e + dpp<QX1>(e);
      Ls += dpp<QX2>(Ls);
      Ls = x32(x16(Ls, false), false);
      const float e0 = dpp<0x00>(e), e1 = dpp<0x55>(e), e2 = dpp<0xaa>(e), e3 = dpp<0xff>(e);
      float z[4];
#pragma unroll
      for (int c = 0; c < 4; ++c) z[c] = (e0 * y[c].x + e1 * y[c].y) + (e2 * y[c].z + e3 * y[c].w);
      const float zz = s16(s32(z[0], z[2]), s32(z[1], z[3]));
      sts<float>(L_RED + v * 256 + lane * 4, zz);
      if (lane == 0 && kp == 0) sts<v2f>(L_LN + st * 8, v2f{M, Ls});
      BAR();
      if (v == 0) {
        const v4f sl = lds<v4f>(L_LN);
        float r0 = 0.f, r1 = 0.f;
#pragma unroll
        for (int j = 0; j < 4; ++j) { r0 += lds<float>(L_RED + j * 512 + lane * 4); r1 += lds<float>(L_RED + j * 512 + 256 + lane * 4); }
        const float Mx = fmaxf(sl.x, sl.z), f0 = __builtin_amdgcn_exp2f(sl.x - Mx), f1 = __builtin_amdgcn_exp2f(sl.z - Mx);
        const float y = (f0 * r0 + f1 * r1) * __builtin_amdgcn_rcpf(f0 * sl.y + f1 * sl.w);
        __builtin_amdgcn_raw_buffer_store_b32(min(pk(y, dpp<QX1>(y)), SENT - 1), rw,
            (lane & 1) ? OOB : WS_B + (L * 7 + cb) * 2048 + (2 * g + hh) * 128 + lane * 2, 0, SC1);
      }
      TSL(11, 19);
    } else if (is_f) {
      if (L == 0) {
        if (v < 7) {
          nap<D_TAIL>();
          const rsrc_t r1 = mk(w1, 448 * 3584), r2 = mk(w2, 1792 * 896);
#pragma unroll
          for (int i = NPRE; i < 8; ++i)
            dma_nt(L_K + (v * 8 + i) * 1024, r1, (lane >> 3) * 3584 + wsrc(lane, i & 1), (v * 8 + i) * 8 * 3584 + f * 128);
#pragma unroll
          for (int i = 0; i < 8; ++i)
            dma_nt(L_V + (v * 8 + i) * 1024, r2, (lane >> 3) * 896 + wsrc(lane, i & 1), (f * 64 + i * 8) * 896 + v * 128);
          xres = asf((u32)xin[v * 64 + (lane & 7) * 8 + (lane >> 3)] << 16);
          WAITV(0);
          const u32 wa = wtr(L_K + v * 8192, lane), wb = wtr(L_V + v * 8192, lane);
#pragma unroll
          for (int i = 0; i < 8; ++i) { wf[i] = wfrag(wa, i >> 2, i & 3); wf[8 + i] = wfrag(wb, i >> 2, i & 3); }
          v4f cs[4];
#pragma unroll
          for (int c = 0; c < 4; ++c) cs[c] = mfma(ONES, wf[4 + c], mfma(ONES, wf[c], Z));
          sts<float>(L_CS + v * 256 + lane * 4, pick(cs[0].x, cs[1].x, cs[2].x, cs[3].x, q));
        }
        BAR();
#pragma unroll
        for (int j = 0; j < 7; ++j) cs1 += lds<float>(L_CS + j * 256 + lane * 4);
        TS(9);
      } else {
        nap<D_B>();
      }
      if (v < 7) {
        const u32 o = WS_B + (L * 7 + v) * 2048 + lane * 16;
        v4u r0 = LD16(o), r1 = LD16(o + 1024);
        for (int sp = 0; sp < kSpin; ++sp) {
          const bool bad = max(mx4(r0), mx4(r1)) == SENT;
          if (!__any(bad)) break;
          nap<D_RT>();
          asm volatile("" ::: "memory");
          if (bad) { r0 = LD16(o); r1 = LD16(o + 1024); }
        }
        TSL(10, 18);
        float s[8] = {0.f, 0.f, 0.f, 0.f, 0.f, 0.f, 0.f, 0.f};
        acc8(s, r0); acc8(s, r1);
        const float xs = xres + rscat(s, lane);
        const u32 xb = pk(xs, xs);
        sts<unsigned short>(L_XS + v * 128 + ((lane & 7) * 8 + (lane >> 3)) * 2, (unsigned short)xb);
        asm volatile("" ::: "memory");
        const v4u a0 = lds<v4u>(L_XS + v * 128 + q * 16), a1 = lds<v4u>(L_XS + v * 128 + 64 + q * 16);
        ln_part(asf(xb << 16), v, lane);
        v4f y[4];
#pragma unroll
        for (int c = 0; c < 4; ++c) y[c] = mfma(a1, wf[4 + c], mfma(a0, wf[c], Z));
        sts<float>(L_RED + v * 256 + lane * 4, pick(y[0].x, y[1].x, y[2].x, y[3].x, q));
        if (f == 0)
          __builtin_amdgcn_raw_buffer_store_b32(min(lds<u32>(L_XS + v * 128 + (lane & 31) * 4), SENT - 1), rw,
              lane < 32 ? WS_C + (L * 7 + v) * 4096 + 28 * 128 + lane * 4 : OOB, 0, SC1);
      }
      BAR();
      if (v < 7) {
        float mean, rstd, r = 0.f;
        ln_fin(mean, rstd);
#pragma unroll
        for (int j = 0; j < 7; ++j) r += lds<float>(L_RED + j * 256 + lane * 4);
        const float av = rbf((r - mean * cs1) * rstd);
        const float hv = av * __builtin_amdgcn_rcpf(1.f + __builtin_amdgcn_exp2f(av * -1.4426950408889634f));
        sts<unsigned short>(L_HS + v * 128 + lane * 2, (unsigned short)pk(hv, hv));
        asm volatile("" ::: "memory");
        const v4u a0 = lds<v4u>(L_HS + v * 128 + q * 16), a1 = lds<v4u>(L_HS + v * 128 + 64 + q * 16);
        v4f y[4];
#pragma unroll
        for (int c = 0; c < 4; ++c) y[c] = mfma(a1, wf[12 + c], mfma(a0, wf[8 + c], Z));
        const float yv = pick(y[0].x, y[1].x, y[2].x, y[3].x, q);
        __builtin_amdgcn_raw_buffer_store_b32(min(pk(yv, dpp<QX1>(yv)), SENT - 1), rw,
            (lane & 1) ? OOB : WS_C + (L * 7 + v) * 4096 + f * 128 + lane * 2, 0, SC1);
      }
      TSL(11, 19);
    }
    if (L && !(m == 20 && v == g)) break;
    if (!is_f) nap<D_C>();
    if (v < 7) {
      const float xs = gather_c(rw, WS_C + (L * 7 + v) * 4096, lane);
      const u32 xb = pk(xs, xs);
      TSL(12, 20);
      if (L) { out[v * 64 + (lane & 7) * 8 + (lane >> 3)] = (unsigned short)xb; break; }
      xres = asf(xb << 16);
      sts<unsigned short>(L_XS + v * 128 + ((lane & 7) * 8 + (lane >> 3)) * 2, (unsigned short)xb);
      asm volatile("" ::: "memory");
      const v4u a0 = lds<v4u>(L_XS + v * 128 + q * 16), a1 = lds<v4u>(L_XS + v * 128 + 64 + q * 16);
      ln_part(xres, v, lane);
      const v4f aq = mfma(a1, qf2[1], mfma(a0, qf2[0], Z));
      if (lane < 16) sts<float>(L_RED + v * 256 + lane * 4, aq.x);
    }
    if (L) break;
    BAR();
    if (v == 0) {
      float mean, rstd, r = 0.f;
      ln_fin(mean, rstd);
#pragma unroll
      for (int j = 0; j < 7; ++j) r += lds<float>(L_RED + j * 256 + n * 4);
      const v2u gq = {asu(rbf((r - mean * csq) * rstd)), tag};
      __builtin_amdgcn_raw_buffer_store_b64(gq, rw, lane < 16 ? WS_Q + ((8 + g) * 32 + m) * 128 + lane * 8 : OOB, 0, SC1);
    }
    TS(13);
    if (v < 4) nap<D_Q1>();
  }
  TSOUT();
}
}

extern "C" void kernel_launch(void* const* d_in, const int* in_sizes, int n_in, void* d_out, int out_size, void* d_ws, size_t ws_size,
                              hipStream_t stream) {
  (void)hipFuncSetAttribute((const void*)fused_kernel, hipFuncAttributeMaxDynamicSharedMemorySize, L_END);
  fused_kernel<<<256, 768, L_END, stream>>>((const unsigned short*)d_in[0], (const char*)d_in[1], (const char*)d_in[2], (const char*)d_in[3],
                                            (const char*)d_in[4], (const char*)d_in[6], (const char*)d_in[7], (unsigned short*)d_out, (char*)d_ws);
}
```

```cpp
#define PI 0
#define PRE 0
#include <hip/hip_runtime.h>
#include <hip/hip_bf16.h>

typedef unsigned u32;
typedef unsigned long long u64;
typedef u32 v4u __attribute__((ext_vector_type(4)));
typedef u32 v2u __attribute__((ext_vector_type(2)));
typedef float v4f __attribute__((ext_vector_type(4)));
typedef float v2f __attribute__((ext_vector_type(2)));
typedef short v4s __attribute__((ext_vector_type(4)));
typedef __bf16 v8bf __attribute__((ext_vector_type(8)));
typedef __bf16 v2bf __attribute__((ext_vector_type(2)));
#define LAS __attribute__((address_space(3)))
typedef __amdgpu_buffer_rsrc_t rsrc_t;

namespace {
constexpr int kSK = 65536, kW = 4096;
constexpr u32 WS_Q = 4096;
constexpr u32 WS_A = WS_Q + 2 * 8 * 32 * 128;
constexpr u32 WS_S = WS_A + 2 * 8 * 32768;
constexpr u32 WS_B = WS_S + 2 * 8 * 8192;
constexpr u32 WS_C = WS_B + 2 * 7 * 2048;
constexpr u32 WS_END = WS_C + 2 * 7 * 4096;
constexpr u32 OOB = 0x80000000u;
constexpr u32 L_QS = 0;
constexpr u32 L_OP = L_QS + 1280;
constexpr u32 L_ST = L_OP + 8192;
constexpr u32 L_XS = L_ST + 64;
constexpr u32 L_HS = L_XS + 896;
constexpr u32 L_RED = L_HS + 896;
constexpr u32 L_LN = L_RED + 2048;
constexpr u32 L_CS = L_LN + 64;
constexpr u32 L_WQ = 16384;
static_assert(L_CS + 1792 <= L_WQ, "lds");
constexpr u32 L_K = 32768, L_V = L_K + 65536, L_END = L_V + 65536;
static_assert(L_END <= 163840, "lds");
constexpr int SC1 = 16;
constexpr u32 SENT = 0xffffffffu;
constexpr int kSpin = 1 << 16;

#define D_Q0 70
#define D_Q1 4
#define D_A 4
#define D_B 30
#define D_C 40
#define D_RT 2
#define D_TAIL 5

__device__ __forceinline__ rsrc_t mk(const void* p, u32 n) { return __builtin_amdgcn_make_buffer_rsrc((void*)p, 0, n, 0x00020000); }
__device__ __forceinline__ void dma_nt(u32 lds, rsrc_t r, u32 voff, u32 soff) {
  asm volatile("s_mov_b32 m0, %0\n\ts_nop 0\n\tbuffer_load_dwordx4 %1, %2, %3 offen nt lds" ::"s"(lds), "v"(voff), "s"(r), "s"(soff) : "memory");
}
__device__ __forceinline__ void dma_sc1(u32 lds, rsrc_t r, u32 voff, u32 soff) {
  asm volatile("s_mov_b32 m0, %0\n\ts_nop 0\n\tbuffer_load_dwordx4 %1, %2, %3 offen sc1 lds" ::"s"(lds), "v"(voff), "s"(r), "s"(soff) : "memory");
}
#define WAITV(n) asm volatile("s_waitcnt vmcnt(" #n ")" ::: "memory")
#define BAR() asm volatile("s_waitcnt lgkmcnt(0)\n\ts_barrier" ::: "memory")
#define WAITL() asm volatile("s_waitcnt lgkmcnt(0)" ::: "memory")
template <int N> __device__ __forceinline__ void nap() {
  if constexpr (N > 127) { __builtin_amdgcn_s_sleep(127); nap<N - 127>(); }
  else if constexpr (N > 0) __builtin_amdgcn_s_sleep(N);
}
__device__ __forceinline__ float asf(u32 x) { return __builtin_bit_cast(float, x); }
__device__ __forceinline__ u32 asu(float x) { return __builtin_bit_cast(u32, x); }
__device__ __forceinline__ v4f mfma(v4u a, v4u b, v4f c) {
  return __builtin_amdgcn_mfma_f32_16x16x32_bf16(__builtin_bit_cast(v8bf, a), __builtin_bit_cast(v8bf, b), c, 0, 0, 0);
}
__device__ __forceinline__ u32 pk(float a, float b) { v2bf c; c.x = (__bf16)a; c.y = (__bf16)b; return __builtin_bit_cast(u32, c); }
__device__ __forceinline__ float rbf(float a) { return asf(pk(a, a) << 16); }
template <typename T> __device__ __forceinline__ T lds(u32 a) { return *(const LAS T*)a; }
template <typename T> __device__ __forceinline__ void sts(u32 a, T v) { *(LAS T*)a = v; }
__device__ __forceinline__ v2u trd(u32 a) { return __builtin_bit_cast(v2u, __builtin_amdgcn_ds_read_tr16_b64_v4i16((LAS v4s*)a)); }
__device__ __forceinline__ v4u tr2(u32 a0, u32 a1) { v2u x = trd(a0), y = trd(a1); return v4u{x.x, x.y, y.x, y.y}; }
template <int C> __device__ __forceinline__ float dpp(float x) { return asf(__builtin_amdgcn_update_dpp(0, asu(x), C, 0xf, 0xf, true)); }
constexpr int ROR8 = 0x128, ROR4 = 0x124, ROR2 = 0x122, ROR1 = 0x121, QX1 = 0xb1, QX2 = 0x4e;
__device__ __forceinline__ float x16(float x, bool mx) { auto r = __builtin_amdgcn_permlane16_swap(asu(x), asu(x), false, false); return mx ? fmaxf(asf(r[0]), asf(r[1])) : asf(r[0]) + asf(r[1]); }
__device__ __forceinline__ float x32(float x, bool mx) { auto r = __builtin_amdgcn_permlane32_swap(asu(x), asu(x), false, false); return mx ? fmaxf(asf(r[0]), asf(r[1])) : asf(r[0]) + asf(r[1]); }
__device__ __forceinline__ float s16(float a, float b) { auto r = __builtin_amdgcn_permlane16_swap(asu(a), asu(b), false, false); return asf(r[0]) + asf(r[1]); }
__device__ __forceinline__ float s32(float a, float b) { auto r = __builtin_amdgcn_permlane32_swap(asu(a), asu(b), false, false); return asf(r[0]) + asf(r[1]); }
__device__ __forceinline__ float wsum(float x) {
  x += dpp<ROR8>(x); x += dpp<ROR4>(x); x += dpp<ROR2>(x); x += dpp<ROR1>(x);
  return x32(x16(x, false), false);
}
__device__ __forceinline__ u32 mx4(v4u a) { return max(max(a.x, a.y), max(a.z, a.w)); }
__device__ __forceinline__ void acc8(float (&s)[8], v4u r) {
#pragma unroll
  for (int j = 0; j < 4; ++j) { s[2 * j] += asf(r[j] << 16); s[2 * j + 1] += asf(r[j] & 0xffff0000u); }
}
__device__ __forceinline__ float rscat(const float (&s)[8], int lane) {
  float t0 = s32(s[0], s[4]), t1 = s32(s[1], s[5]), t2 = s32(s[2], s[6]), t3 = s32(s[3], s[7]);
  float u0 = s16(t0, t2), u1 = s16(t1, t3);
  const bool b3 = lane & 8;
  return (b3 ? u1 : u0) + dpp<ROR8>(b3 ? u0 : u1);
}
__device__ __forceinline__ void ln_part(float x, int w, int lane) {
  const float x0 = asf(__builtin_amdgcn_readfirstlane(asu(x)));
  const float d = x - x0;
  const float s1 = wsum(d), s2 = wsum(d * d);
  if (lane == 0) sts<v2f>(L_LN + w * 8, v2f{x0 + s1 * (1.f / 64), s2 - s1 * s1 * (1.f / 64)});
}
__device__ __forceinline__ void ln_fin(float& mean, float& rstd) {
  const v4f a = lds<v4f>(L_LN), b = lds<v4f>(L_LN + 16), c = lds<v4f>(L_LN + 32);
  const v2f d = lds<v2f>(L_LN + 48);
  mean = (((a.x + a.z) + (b.x + b.z)) + ((c.x + c.z) + d.x)) * (1.f / 7);
  const float e0 = a.x - mean, e1 = a.z - mean, e2 = b.x - mean, e3 = b.z - mean, e4 = c.x - mean, e5 = c.z - mean, e6 = d.x - mean;
  const float m2 = (((a.y + a.w) + (b.y + b.w)) + ((c.y + c.w) + d.y)) + 64.f * ((e0 * e0 + e1 * e1 + e2 * e2 + e3 * e3) + (e4 * e4 + e5 * e5 + e6 * e6));
  rstd = __builtin_amdgcn_rsqf(m2 * (1.f / 448) + 1e-5f);
}
__device__ __forceinline__ float pick(float a, float b, float c, float d, int q) {
  const float lo = (q & 1) ? b : a, hi = (q & 1) ? d : c;
  return (q & 2) ? hi : lo;
}
__device__ __forceinline__ u32 wtr(u32 base, int lane) {
  const int G = lane >> 4, r = (lane >> 2) & 3, p = lane & 3;
  return base + (8 * G + r) * 128 + ((((r >> 1) & 1) | ((G & 1) << 1)) << 5) + p * 8;
}
__device__ __forceinline__ v4u wfrag(u32 a, int ks, int ct) { const u32 b = (a ^ (ct << 5)) + ks * 4096; return tr2(b, b + 512); }
__device__ __forceinline__ u32 wsrc(int lane, int odd) { return (u32)(((lane & 7) ^ ((((lane >> 4) & 1) | (odd << 1)) << 1)) << 4); }

#define TS(i) do { if ((i) == 0 || (i) == 20) __builtin_amdgcn_raw_buffer_store_b32((u32)__builtin_amdgcn_s_memrealtime(), mk(ws + (3u << 20), 1 << 16), (threadIdx.x & 63) == 0 ? ((i) ? 16384 : 0) + (blockIdx.x * 12 + (threadIdx.x >> 6)) * 4 : OOB, 0, SC1); } while (0)
#define TSL(i, j) do { if (L) TS(j); else TS(i); } while (0)
#define TSOUT()
__global__ void k_pre(char* ws) { if (threadIdx.x == 0) *(u32*)(ws + (3u << 20) + 65536) = (u32)__builtin_amdgcn_s_memrealtime(); }
__global__ void k_post(char* ws) {
 const u32 tz = (u32)__builtin_amdgcn_s_memrealtime(); const int lane = threadIdx.x;
 u32 mn = ~0u, mxx = 0;
 for (int i = lane; i < 3072; i += 256) { u32 t = ((u32*)(ws + (3u << 20)))[i]; mn = min(mn, tz - t > 100000u ? ~0u : t);
   u32 t2 = ((u32*)(ws + (3u << 20)))[4096 + i]; mxx = max(mxx, tz - t2 > 100000u ? 0u : t2); }
 __shared__ u32 a[256], b[256]; a[lane] = mn; b[lane] = mxx; __syncthreads();
 for (int i = 0; i < 256; ++i) { mn = min(mn, a[i]); mxx = max(mxx, b[i]); }
 const u32 ta = *(u32*)(ws + (3u << 20) + 65536);
 const u32 dl = PI == 0 ? 0u : PI == 1 ? 4 * (mn - ta) : PI == 2 ? 4 * (tz - mxx) : 4 * (mxx - mn);
 while ((u32)__builtin_amdgcn_s_memrealtime() - tz < min(dl, 20000u) + 500u) {} }

#define NPRE 4
#define LD16(o) __builtin_amdgcn_raw_buffer_load_b128(rw, (o), 0, SC1)

__device__ __forceinline__ float gather_c(rsrc_t rw, u32 base, int lane) {
  const u32 o = base + lane * 16, o3 = lane < 40 ? o + 3072 : OOB;
  v4u r0 = LD16(o), r1 = LD16(o + 1024), r2 = LD16(o + 2048), r3 = LD16(o3);
  for (int sp = 0; sp < kSpin; ++sp) {
    const bool bad = max(max(mx4(r0), mx4(r1)), max(mx4(r2), mx4(r3))) == SENT;
    if (!__any(bad)) break;
    nap<D_RT>();
    asm volatile("" ::: "memory");
    if (bad) { r0 = LD16(o); r1 = LD16(o + 1024); r2 = LD16(o + 2048); r3 = LD16(o3); }
  }
  float s[8] = {0.f, 0.f, 0.f, 0.f, 0.f, 0.f, 0.f, 0.f};
  acc8(s, r0); acc8(s, r1); acc8(s, r2); acc8(s, r3);
  return rscat(s, lane);
}

__global__ void __launch_bounds__(768, 3)
fused_kernel(const unsigned short* __restrict__ xin, const char* __restrict__ Kc, const char* __restrict__ Vc, const char* __restrict__ wq,
             const char* __restrict__ wo, const char* __restrict__ w1, const char* __restrict__ w2, unsigned short* __restrict__ out,
             char* __restrict__ ws) {
  TS(0);
  const int lane = threadIdx.x & 63, w = __builtin_amdgcn_readfirstlane(threadIdx.x >> 6);
  const int g = blockIdx.x & 7, m = blockIdx.x >> 3;
  const int n = lane & 15, q = lane >> 4;
  const bool is_o = m < 14, is_f = m >= 28 && (m - 28) * 8 + g < 28;
  const rsrc_t rw = mk(ws, WS_END);
  const v4f Z = {0.f, 0.f, 0.f, 0.f};

  if (w < 4) {
    const int a = w;
    v4u kf[16], vf[16];
    {
      const size_t cu = ((size_t)g * kSK + (kSK - kW) + m * 128) * 512;
      const rsrc_t rk = mk(Kc + cu, 65536), rv = mk(Vc + cu, 65536);
      const int hi = lane >> 5, p = lane & 31;
      const u32 vk = hi * 512 + ((p ^ hi) << 4), vv = hi * 512 + ((p ^ (hi << 1)) << 4);
#pragma unroll
      for (int i = 0; i < 16; ++i) dma_nt(L_K + a * 16384 + i * 1024, rk, vk ^ ((2 * i & 15) << 4), (((i + m) & 15) * 4 + a) * 1024);
#pragma unroll
      for (int i = 0; i < 16; ++i) dma_nt(L_V + a * 16384 + i * 1024, rv, vv ^ ((2 * i & 7) << 5), (((i + m) & 15) * 4 + a) * 1024);
      TS(1);
      WAITV(16);
      const u32 ka = L_K + a * 16384 + n * 512 + ((q ^ n) << 4);
#pragma unroll
      for (int i = 0; i < 16; ++i) kf[i] = lds<v4u>((ka ^ ((i & 3) << 6)) + ((i >> 2) & 1) * 256 + (i >> 3) * 8192);
      TS(2);
    }
#pragma unroll
    for (int L = 0; L < 2; ++L) {
      BAR();
      TSL(5, 15);
      v4f sc0 = Z, sc1 = Z;
#pragma unroll
      for (int i = 0; i < 8; ++i) {
        const v4u b = lds<v4u>(L_QS + (n & 1) * 576 + i * 64 + q * 16);
        sc0 = mfma(kf[i], b, sc0);
        sc1 = mfma(kf[8 + i], b, sc1);
      }
      constexpr float c = 1.4426950408889634f / 16;
      float mx = fmaxf(fmaxf(fmaxf(sc0.x, sc0.y), fmaxf(sc0.z, sc0.w)), fmaxf(fmaxf(sc1.x, sc1.y), fmaxf(sc1.z, sc1.w)));
      mx = x32(x16(mx, true), true) * c;
      v4f p0, p1;
#pragma unroll
      for (int j = 0; j < 4; ++j) { p0[j] = __builtin_amdgcn_exp2f(sc0[j] * c - mx); p1[j] = __builtin_amdgcn_exp2f(sc1[j] * c - mx); }
      const v4u P = {pk(p0.x, p0.y), pk(p0.z, p0.w), pk(p1.x, p1.y), pk(p1.z, p1.w)};
      const float l = x32(x16(((p0.x + p0.y) + (p0.z + p0.w)) + ((p1.x + p1.y) + (p1.z + p1.w)), false), false);
      if (lane < 2) { sts<float>(L_ST + lane * 32 + a * 4, mx); sts<float>(L_ST + lane * 32 + 16 + a * 4, l); }
      if (L == 0) {
        const int r = (lane >> 2) & 3, p = lane & 3;
        const u32 va = L_V + a * 16384 + (4 * q + r) * 512 + ((4 * (q & 1) + r) << 5) + p * 8;
        TS(6);
        WAITV(8);
#pragma unroll
        for (int i = 0; i < 16; ++i) { const v2u x = trd((va ^ ((i & 7) << 5)) + (i >> 3) * 256); vf[i].x = x.x; vf[i].y = x.y; }
        WAITV(0);
        TS(7);
#pragma unroll
        for (int t = 0; t < 8; ++t)
#pragma unroll
          for (int T = 0; T < 2; ++T) { const v2u x = trd((va ^ (t << 5)) + T * 256 + 8192); vf[8 * T + t].z = x.x; vf[8 * T + t].w = x.y; }
      }
      v4f o0 = Z, o1 = Z;
#pragma unroll
      for (int t = 0; t < 8; ++t) {
        const bool on = (n >> 1) == t;
        const v4u A = {on ? P.x : 0u, on ? P.y : 0u, on ? P.z : 0u, on ? P.w : 0u};
        o0 = mfma(A, vf[t], o0);
        o1 = mfma(A, vf[8 + t], o1);
      }
      const u32 oa = L_OP + a * 2048 + (32 * q + n) * 4;
#pragma unroll
      for (int r = 0; r < 4; ++r) {
        sts<float>(oa + (r & 1) * 1024 + (r >> 1) * 64, o0[r]);
        sts<float>(oa + (r & 1) * 1024 + (r >> 1) * 64 + 512, o1[r]);
      }
      BAR();
      TSL(8, 16);
      {
        const int h = a >> 1, e = 64 * (a & 1) + lane;
        const v4f ms = lds<v4f>(L_ST + h * 32), ls = lds<v4f>(L_ST + h * 32 + 16);
        v2f ov[4];
#pragma unroll
        for (int j = 0; j < 4; ++j) ov[j] = lds<v2f>(L_OP + j * 2048 + h * 1024 + e * 8);
        const float M = fmaxf(fmaxf(ms.x, ms.y), fmaxf(ms.z, ms.w));
        v4f fa;
#pragma unroll
        for (int j = 0; j < 4; ++j) fa[j] = __builtin_amdgcn_exp2f(ms[j] - M);
        const float Ls = (fa.x * ls.x + fa.y * ls.y) + (fa.z * ls.z + fa.w * ls.w);
        const float inv = __builtin_amdgcn_rcpf(Ls);
        const float y0 = ((fa.x * ov[0].x + fa.y * ov[1].x) + (fa.z * ov[2].x + fa.w * ov[3].x)) * inv;
        const float y1 = ((fa.x * ov[0].y + fa.y * ov[1].y) + (fa.z * ov[2].y + fa.w * ov[3].y)) * inv;
        __builtin_amdgcn_raw_buffer_store_b32(min(pk(y0, y1), SENT - 1), rw,
            WS_A + h * 16384 + (e >> 5) * 4096 + m * 128 + (((((e >> 2) & 7) ^ ((m >> 1) & 7)) << 4) | ((e & 3) << 2)), (L * 8 + g) * 32768, SC1);
        if (!(a & 1))
          __builtin_amdgcn_raw_buffer_store_b32(min(asu(M + __builtin_amdgcn_logf(Ls)), SENT - 1), rw,
              lane < 32 ? WS_S + h * 4096 + m * 128 + lane * 4 : OOB, (L * 8 + g) * 8192, SC1);
      }
      TSL(9, 17);
      if (L) break;
      if (is_o || is_f) { BAR(); BAR(); }
      BAR();
    }
    TSOUT();
    return;
  }

  const int v = w - 4;
  const int f = (m - 28) * 8 + g, hh = m & 1, cb = m >> 1;
  const v4u ONES = {0x3f803f80u, 0x3f803f80u, 0x3f803f80u, 0x3f803f80u};
  v4u wf[16];
  v4u qf2[2];
  float csq = 0.f, cs1 = 0.f, xres = 0.f;
  __builtin_amdgcn_s_setprio(3);
  const u32 tag = *(const u32*)ws * 0x9e3779b5u + 0x7f4a7c15u;
  if (v == 0) {
    const v4u S = {SENT, SENT, SENT, SENT};
    const u32 l8 = lane >> 3, c16 = (lane & 7) * 16;
    __builtin_amdgcn_raw_buffer_store_b128(S, rw, WS_A + l8 * 4096 + m * 128 + c16, g * 32768, SC1);
    __builtin_amdgcn_raw_buffer_store_b128(S, rw, WS_A + l8 * 4096 + m * 128 + c16, (8 + g) * 32768, SC1);
    u32 o3 = OOB;
    if (l8 < 4) o3 = WS_S + ((l8 >> 1) * 8 + g) * 8192 + (l8 & 1) * 4096 + m * 128 + c16;
    else if (l8 < 6 && is_o) o3 = WS_B + ((l8 & 1) * 7 + cb) * 2048 + (2 * g + hh) * 128 + c16;
    __builtin_amdgcn_raw_buffer_store_b128(S, rw, o3, 0, SC1);
    if (is_f) {
      const u32 o4 = l8 < 7 ? WS_C + l8 * 4096 + f * 128 + c16 : OOB;
      __builtin_amdgcn_raw_buffer_store_b128(S, rw, o4, 0, SC1);
      __builtin_amdgcn_raw_buffer_store_b128(S, rw, o4, 7 * 4096, SC1);
      if (f == 0) {
        __builtin_amdgcn_raw_buffer_store_b128(S, rw, o4, 28 * 128, SC1);
        __builtin_amdgcn_raw_buffer_store_b128(S, rw, o4, 7 * 4096 + 28 * 128, SC1);
      }
    }
    v4u xv;
    asm volatile("buffer_load_dwordx4 %0, %1, %2, 0 offen" : "=v"(xv) : "v"(lane * 16), "s"(mk(xin, 896)) : "memory");
    const rsrc_t rq = mk(wq, 448 * 8192);
    const u32 vq = (lane >> 1) * 8192 + (g * 512 + m * 16) * 2 + (lane & 1) * 16;
#pragma unroll
    for (int i = 0; i < 14; ++i) dma_nt(L_WQ + i * 1024, rq, vq, i * 32 * 8192);
    TS(1);
    asm volatile("s_waitcnt vmcnt(14)" : "+v"(xv)::"memory");
    float xs[8] = {0.f, 0.f, 0.f, 0.f, 0.f, 0.f, 0.f, 0.f};
    acc8(xs, xv);
    if (lane < 56) sts<v4u>(L_XS + lane * 16, xv);
    const float mean = wsum(((xs[0] + xs[1]) + (xs[2] + xs[3])) + ((xs[4] + xs[5]) + (xs[6] + xs[7]))) * (1.f / 448);
    float m2 = 0.f;
#pragma unroll
    for (int j = 0; j < 8; ++j) m2 += (xs[j] - mean) * (xs[j] - mean);
    const float rstd = __builtin_amdgcn_rsqf(wsum(lane < 56 ? m2 : 0.f) * (1.f / 448) + 1e-5f);
    WAITV(0);
    TS(2);
    v4f aq = Z, as = Z;
    const u32 qa = L_WQ + (8 * q + ((lane >> 2) & 3)) * 32 + (lane & 3) * 8;
#pragma unroll
    for (int i = 0; i < 14; ++i) {
      const v4u b = tr2(qa + i * 1024, qa + i * 1024 + 128);
      aq = mfma(lds<v4u>(L_XS + i * 64 + q * 16), b, aq);
      as = mfma(ONES, b, as);
    }
    csq = as.x;
    const v2u gq = {asu(rbf((aq.x - mean * csq) * rstd)), tag};
    __builtin_amdgcn_raw_buffer_store_b64(gq, rw, lane < 16 ? WS_Q + (g * 32 + m) * 128 + lane * 8 : OOB, 0, SC1);
    TS(3);
    nap<D_Q0 / 3>();
  } else if (v < 4) {
    nap<D_Q0>();
  }

#pragma unroll
  for (int L = 0; L < 2; ++L) {
    if (v < 4) {
      const u32 o = WS_Q + (L * 8 + g) * 4096 + v * 1024 + lane * 16, of = L ? o : WS_Q + (v * 64 + lane) * 128;
      v4u r = LD16(o), fl = r;
      if (L == 0) fl = LD16(of);
      for (int sp = 0; sp < kSpin; ++sp) {
        const bool bad = r.y != tag || r.w != tag || fl.y != tag || fl.w != tag;
        if (!__any(bad)) break;
        nap<D_RT>();
        asm volatile("" ::: "memory");
        if (bad) { r = LD16(o); fl = r; if (L == 0) fl = LD16(of); }
      }
      sts<u32>(L_QS + (v >> 1) * 576 + (v & 1) * 256 + lane * 4, (r.x >> 16) | (r.z & 0xffff0000u));
    }
    TSL(4, 14);
    BAR();
    TSL(5, 15);
    if (L == 0) {
      if (blockIdx.x == 0 && v == 1) __builtin_amdgcn_raw_buffer_store_b32(tag, rw, lane == 0 ? 0 : OOB, 0, SC1);
      __builtin_amdgcn_s_setprio(0);
      if (is_o) {
        const rsrc_t ro = mk(wo, 4096 * 896);
#pragma unroll
        for (int i = 0; i < 4; ++i)
          dma_nt(L_K + (v * 4 + i) * 1024, ro, (lane >> 3) * 896 + wsrc(lane, i & 1), ((2 * g + hh) * 256 + (v * 4 + i) * 8) * 896 + cb * 128);
      } else if (is_f && v < 7) {
        const rsrc_t r1 = mk(w1, 448 * 3584);
#pragma unroll
        for (int i = 0; i < NPRE; ++i)
          dma_nt(L_K + (v * 8 + i) * 1024, r1, (lane >> 3) * 3584 + wsrc(lane, i & 1), (v * 8 + i) * 8 * 3584 + f * 128);
      }
      if (v < 7) {
        const u32 qa = L_WQ + v * 2048 + (8 * q + ((lane >> 2) & 3)) * 32 + (lane & 3) * 8;
        qf2[0] = tr2(qa, qa + 128);
        qf2[1] = tr2(qa + 1024, qa + 1024 + 128);
      }
    }
    BAR();
    TSL(8, 16);
    if (is_o) {
      const int kp = v >> 1, st = v & 1;
      const u32 sa = WS_A + (L * 8 + g) * 32768 + hh * 16384 + kp * 4096 + st * 2048;
      const u32 so = WS_S + (L * 8 + g) * 8192 + hh * 4096 + (16 * st + 4 * q + (lane & 3)) * 128;
      const u32 stg = L_V + v * 2048, aa = stg + n * 128 + ((q ^ (n >> 1)) << 4);
      if (L == 0) {
        WAITV(0);
        BAR();
        const u32 wa = wtr(L_K + kp * 8192, lane);
#pragma unroll
        for (int i = 0; i < 8; ++i) wf[i] = wfrag(wa, i >> 2, i & 3);
      }
      nap<D_A>();
      v4u a0, a1;
      u32 lse;
      for (int sp = 0; sp < kSpin; ++sp) {
        dma_sc1(stg, rw, lane * 16, sa);
        dma_sc1(stg + 1024, rw, lane * 16, sa + 1024);
        lse = __builtin_amdgcn_raw_buffer_load_b32(rw, so, 0, SC1);
        WAITV(0);
        a0 = lds<v4u>(aa); a1 = lds<v4u>(aa ^ 64);
        if (!__any(max(max(mx4(a0), mx4(a1)), lse) == SENT)) break;
        nap<D_RT>();
      }
      TSL(10, 18);
      v4f y[4];
#pragma unroll
      for (int c = 0; c < 4; ++c) y[c] = mfma(a1, wf[4 + c], mfma(a0, wf[c], Z));
      float M = asf(lse);
      M = fmaxf(M, dpp<QX1>(M)); M = fmaxf(M, dpp<QX2>(M));
      M = x32(x16(M, true), true);
      const float e = __builtin_amdgcn_exp2f(asf(lse) - M);
      float Ls = e + dpp<QX1>(e);
      Ls += dpp<QX2>(Ls);
      Ls = x32(x16(Ls, false), false);
      const float e0 = dpp<0x00>(e), e1 = dpp<0x55>(e), e2 = dpp<0xaa>(e), e3 = dpp<0xff>(e);
      float z[4];
#pragma unroll
      for (int c = 0; c < 4; ++c) z[c] = (e0 * y[c].x + e1 * y[c].y) + (e2 * y[c].z + e3 * y[c].w);
      const float zz = s16(s32(z[0], z[2]), s32(z[1], z[3]));
      sts<float>(L_RED + v * 256 + lane * 4, zz);
      if (lane == 0 && kp == 0) sts<v2f>(L_LN + st * 8, v2f{M, Ls});
      BAR();
      if (v == 0) {
        const v4f sl = lds<v4f>(L_LN);
        float r0 = 0.f, r1 = 0.f;
#pragma unroll
        for (int j = 0; j < 4; ++j) { r0 += lds<float>(L_RED + j * 512 + lane * 4); r1 += lds<float>(L_RED + j * 512 + 256 + lane * 4); }
        const float Mx = fmaxf(sl.x, sl.z), f0 = __builtin_amdgcn_exp2f(sl.x - Mx), f1 = __builtin_amdgcn_exp2f(sl.z - Mx);
        const float y = (f0 * r0 + f1 * r1) * __builtin_amdgcn_rcpf(f0 * sl.y + f1 * sl.w);
        __builtin_amdgcn_raw_buffer_store_b32(min(pk(y, dpp<QX1>(y)), SENT - 1), rw,
            (lane & 1) ? OOB : WS_B + (L * 7 + cb) * 2048 + (2 * g + hh) * 128 + lane * 2, 0, SC1);
      }
      TSL(11, 19);
    } else if (is_f) {
      if (L == 0) {
        if (v < 7) {
          nap<D_TAIL>();
          const rsrc_t r1 = mk(w1, 448 * 3584), r2 = mk(w2, 1792 * 896);
#pragma unroll
          for (int i = NPRE; i < 8; ++i)
            dma_nt(L_K + (v * 8 + i) * 1024, r1, (lane >> 3) * 3584 + wsrc(lane, i & 1), (v * 8 + i) * 8 * 3584 + f * 128);
#pragma unroll
          for (int i = 0; i < 8; ++i)
            dma_nt(L_V + (v * 8 + i) * 1024, r2, (lane >> 3) * 896 + wsrc(lane, i & 1), (f * 64 + i * 8) * 896 + v * 128);
          xres = asf((u32)xin[v * 64 + (lane & 7) * 8 + (lane >> 3)] << 16);
          WAITV(0);
          const u32 wa = wtr(L_K + v * 8192, lane), wb = wtr(L_V + v * 8192, lane);
#pragma unroll
          for (int i = 0; i < 8; ++i) { wf[i] = wfrag(wa, i >> 2, i & 3); wf[8 + i] = wfrag(wb, i >> 2, i & 3); }
          v4f cs[4];
#pragma unroll
          for (int c = 0; c < 4; ++c) cs[c] = mfma(ONES, wf[4 + c], mfma(ONES, wf[c], Z));
          sts<float>(L_CS + v * 256 + lane * 4, pick(cs[0].x, cs[1].x, cs[2].x, cs[3].x, q));
        }
        BAR();
#pragma unroll
        for (int j = 0; j < 7; ++j) cs1 += lds<float>(L_CS + j * 256 + lane * 4);
        TS(9);
      } else {
        nap<D_B>();
      }
      if (v < 7) {
        const u32 o = WS_B + (L * 7 + v) * 2048 + lane * 16;
        v4u r0 = LD16(o), r1 = LD16(o + 1024);
        for (int sp = 0; sp < kSpin; ++sp) {
          const bool bad = max(mx4(r0), mx4(r1)) == SENT;
          if (!__any(bad)) break;
          nap<D_RT>();
          asm volatile("" ::: "memory");
          if (bad) { r0 = LD16(o); r1 = LD16(o + 1024); }
        }
        TSL(10, 18);
        float s[8] = {0.f, 0.f, 0.f, 0.f, 0.f, 0.f, 0.f, 0.f};
        acc8(s, r0); acc8(s, r1);
        const float xs = xres + rscat(s, lane);
        const u32 xb = pk(xs, xs);
        sts<unsigned short>(L_XS + v * 128 + ((lane & 7) * 8 + (lane >> 3)) * 2, (unsigned short)xb);
        asm volatile("" ::: "memory");
        const v4u a0 = lds<v4u>(L_XS + v * 128 + q * 16), a1 = lds<v4u>(L_XS + v * 128 + 64 + q * 16);
        ln_part(asf(xb << 16), v, lane);
        v4f y[4];
#pragma unroll
        for (int c = 0; c < 4; ++c) y[c] = mfma(a1, wf[4 + c], mfma(a0, wf[c], Z));
        sts<float>(L_RED + v * 256 + lane * 4, pick(y[0].x, y[1].x, y[2].x, y[3].x, q));
        if (f == 0)
          __builtin_amdgcn_raw_buffer_store_b32(min(lds<u32>(L_XS + v * 128 + (lane & 31) * 4), SENT - 1), rw,
              lane < 32 ? WS_C + (L * 7 + v) * 4096 + 28 * 128 + lane * 4 : OOB, 0, SC1);
      }
      BAR();
      if (v < 7) {
        float mean, rstd, r = 0.f;
        ln_fin(mean, rstd);
#pragma unroll
        for (int j = 0; j < 7; ++j) r += lds<float>(L_RED + j * 256 + lane * 4);
        const float av = rbf((r - mean * cs1) * rstd);
        const float hv = av * __builtin_amdgcn_rcpf(1.f + __builtin_amdgcn_exp2f(av * -1.4426950408889634f));
        sts<unsigned short>(L_HS + v * 128 + lane * 2, (unsigned short)pk(hv, hv));
        asm volatile("" ::: "memory");
        const v4u a0 = lds<v4u>(L_HS + v * 128 + q * 16), a1 = lds<v4u>(L_HS + v * 128 + 64 + q * 16);
        v4f y[4];
#pragma unroll
        for (int c = 0; c < 4; ++c) y[c] = mfma(a1, wf[12 + c], mfma(a0, wf[8 + c], Z));
        const float yv = pick(y[0].x, y[1].x, y[2].x, y[3].x, q);
        __builtin_amdgcn_raw_buffer_store_b32(min(pk(yv, dpp<QX1>(yv)), SENT - 1), rw,
            (lane & 1) ? OOB : WS_C + (L * 7 + v) * 4096 + f * 128 + lane * 2, 0, SC1);
      }
      TSL(11, 19);
    }
    if (L && !(m == 20 && v == g)) break;
    if (!is_f) nap<D_C>();
    if (v < 7) {
      const float xs = gather_c(rw, WS_C + (L * 7 + v) * 4096, lane);
      const u32 xb = pk(xs, xs);
      TSL(12, 20);
      if (L) { out[v * 64 + (lane & 7) * 8 + (lane >> 3)] = (unsigned short)xb; break; }
      xres = asf(xb << 16);
      sts<unsigned short>(L_XS + v * 128 + ((lane & 7) * 8 + (lane >> 3)) * 2, (unsigned short)xb);
      asm volatile("" ::: "memory");
      const v4u a0 = lds<v4u>(L_XS + v * 128 + q * 16), a1 = lds<v4u>(L_XS + v * 128 + 64 + q * 16);
      ln_part(xres, v, lane);
      const v4f aq = mfma(a1, qf2[1], mfma(a0, qf2[0], Z));
      if (lane < 16) sts<float>(L_RED + v * 256 + lane * 4, aq.x);
    }
    if (L) break;
    BAR();
    if (v == 0) {
      float mean, rstd, r = 0.f;
      ln_fin(mean, rstd);
#pragma unroll
      for (int j = 0; j < 7; ++j) r += lds<float>(L_RED + j * 256 + n * 4);
      const v2u gq = {asu(rbf((r - mean * csq) * rstd)), tag};
      __builtin_amdgcn_raw_buffer_store_b64(gq, rw, lane < 16 ? WS_Q + ((8 + g) * 32 + m) * 128 + lane * 8 : OOB, 0, SC1);
    }
    TS(13);
    if (v < 4) nap<D_Q1>();
  }
  TSOUT();
}
}

extern "C" void kernel_launch(void* const* d_in, const int* in_sizes, int n_in, void* d_out, int out_size, void* d_ws, size_t ws_size,
                              hipStream_t stream) {
  (void)hipFuncSetAttribute((const void*)fused_kernel, hipFuncAttributeMaxDynamicSharedMemorySize, L_END);
  fused_kernel<<<256, 768, L_END, stream>>>((const unsigned short*)d_in[0], (const char*)d_in[1], (const char*)d_in[2], (const char*)d_in[3],
                                            (const char*)d_in[4], (const char*)d_in[6], (const char*)d_in[7], (unsigned short*)d_out, (char*)d_ws);
  k_post<<<1, 256, 0, stream>>>((char*)d_ws);
}
```

```cpp
#define PI 0
#define PNB 1
#define TNB 1
#define TNT 64
#define TLDS 0
#define TVG 0
#include <hip/hip_runtime.h>
#include <hip/hip_bf16.h>

typedef unsigned u32;
typedef unsigned long long u64;
typedef u32 v4u __attribute__((ext_vector_type(4)));
typedef u32 v2u __attribute__((ext_vector_type(2)));
typedef float v4f __attribute__((ext_vector_type(4)));
typedef float v2f __attribute__((ext_vector_type(2)));
typedef short v4s __attribute__((ext_vector_type(4)));
typedef __bf16 v8bf __attribute__((ext_vector_type(8)));
typedef __bf16 v2bf __attribute__((ext_vector_type(2)));
#define LAS __attribute__((address_space(3)))
typedef __amdgpu_buffer_rsrc_t rsrc_t;

namespace {
constexpr int kSK = 65536, kW = 4096;
constexpr u32 WS_Q = 4096;
constexpr u32 WS_A = WS_Q + 2 * 8 * 32 * 128;
constexpr u32 WS_S = WS_A + 2 * 8 * 32768;
constexpr u32 WS_B = WS_S + 2 * 8 * 8192;
constexpr u32 WS_C = WS_B + 2 * 7 * 2048;
constexpr u32 WS_END = WS_C + 2 * 7 * 4096;
constexpr u32 OOB = 0x80000000u;
constexpr u32 L_QS = 0;
constexpr u32 L_OP = L_QS + 1280;
constexpr u32 L_ST = L_OP + 8192;
constexpr u32 L_XS = L_ST + 64;
constexpr u32 L_HS = L_XS + 896;
constexpr u32 L_RED = L_HS + 896;
constexpr u32 L_LN = L_RED + 2048;
constexpr u32 L_CS = L_LN + 64;
constexpr u32 L_WQ = 16384;
static_assert(L_CS + 1792 <= L_WQ, "lds");
constexpr u32 L_K = 32768, L_V = L_K + 65536, L_END = L_V + 65536;
static_assert(L_END <= 163840, "lds");
constexpr int SC1 = 16;
constexpr u32 SENT = 0xffffffffu;
constexpr int kSpin = 1 << 16;

#define D_Q0 70
#define D_Q1 4
#define D_A 4
#define D_B 30
#define D_C 40
#define D_RT 2
#define D_TAIL 5

__device__ __forceinline__ rsrc_t mk(const void* p, u32 n) { return __builtin_amdgcn_make_buffer_rsrc((void*)p, 0, n, 0x00020000); }
__device__ __forceinline__ void dma_nt(u32 lds, rsrc_t r, u32 voff, u32 soff) {
  asm volatile("s_mov_b32 m0, %0\n\ts_nop 0\n\tbuffer_load_dwordx4 %1, %2, %3 offen nt lds" ::"s"(lds), "v"(voff), "s"(r), "s"(soff) : "memory");
}
__device__ __forceinline__ void dma_sc1(u32 lds, rsrc_t r, u32 voff, u32 soff) {
  asm volatile("s_mov_b32 m0, %0\n\ts_nop 0\n\tbuffer_load_dwordx4 %1, %2, %3 offen sc1 lds" ::"s"(lds), "v"(voff), "s"(r), "s"(soff) : "memory");
}
#define WAITV(n) asm volatile("s_waitcnt vmcnt(" #n ")" ::: "memory")
#define BAR() asm volatile("s_waitcnt lgkmcnt(0)\n\ts_barrier" ::: "memory")
#define WAITL() asm volatile("s_waitcnt lgkmcnt(0)" ::: "memory")
template <int N> __device__ __forceinline__ void nap() {
  if constexpr (N > 127) { __builtin_amdgcn_s_sleep(127); nap<N - 127>(); }
  else if constexpr (N > 0) __builtin_amdgcn_s_sleep(N);
}
__device__ __forceinline__ float asf(u32 x) { return __builtin_bit_cast(float, x); }
__device__ __forceinline__ u32 asu(float x) { return __builtin_bit_cast(u32, x); }
__device__ __forceinline__ v4f mfma(v4u a, v4u b, v4f c) {
  return __builtin_amdgcn_mfma_f32_16x16x32_bf16(__builtin_bit_cast(v8bf, a), __builtin_bit_cast(v8bf, b), c, 0, 0, 0);
}
__device__ __forceinline__ u32 pk(float a, float b) { v2bf c; c.x = (__bf16)a; c.y = (__bf16)b; return __builtin_bit_cast(u32, c); }
__device__ __forceinline__ float rbf(float a) { return asf(pk(a, a) << 16); }
template <typename T> __device__ __forceinline__ T lds(u32 a) { return *(const LAS T*)a; }
template <typename T> __device__ __forceinline__ void sts(u32 a, T v) { *(LAS T*)a = v; }
__device__ __forceinline__ v2u trd(u32 a) { return __builtin_bit_cast(v2u, __builtin_amdgcn_ds_read_tr16_b64_v4i16((LAS v4s*)a)); }
__device__ __forceinline__ v4u tr2(u32 a0, u32 a1) { v2u x = trd(a0), y = trd(a1); return v4u{x.x, x.y, y.x, y.y}; }
template <int C> __device__ __forceinline__ float dpp(float x) { return asf(__builtin_amdgcn_update_dpp(0, asu(x), C, 0xf, 0xf, true)); }
constexpr int ROR8 = 0x128, ROR4 = 0x124, ROR2 = 0x122, ROR1 = 0x121, QX1 = 0xb1, QX2 = 0x4e;
__device__ __forceinline__ float x16(float x, bool mx) { auto r = __builtin_amdgcn_permlane16_swap(asu(x), asu(x), false, false); return mx ? fmaxf(asf(r[0]), asf(r[1])) : asf(r[0]) + asf(r[1]); }
__device__ __forceinline__ float x32(float x, bool mx) { auto r = __builtin_amdgcn_permlane32_swap(asu(x), asu(x), false, false); return mx ? fmaxf(asf(r[0]), asf(r[1])) : asf(r[0]) + asf(r[1]); }
__device__ __forceinline__ float s16(float a, float b) { auto r = __builtin_amdgcn_permlane16_swap(asu(a), asu(b), false, false); return asf(r[0]) + asf(r[1]); }
__device__ __forceinline__ float s32(float a, float b) { auto r = __builtin_amdgcn_permlane32_swap(asu(a), asu(b), false, false); return asf(r[0]) + asf(r[1]); }
__device__ __forceinline__ float wsum(float x) {
  x += dpp<ROR8>(x); x += dpp<ROR4>(x); x += dpp<ROR2>(x); x += dpp<ROR1>(x);
  return x32(x16(x, false), false);
}
__device__ __forceinline__ u32 mx4(v4u a) { return max(max(a.x, a.y), max(a.z, a.w)); }
__device__ __forceinline__ void acc8(float (&s)[8], v4u r) {
#pragma unroll
  for (int j = 0; j < 4; ++j) { s[2 * j] += asf(r[j] << 16); s[2 * j + 1] += asf(r[j] & 0xffff0000u); }
}
__device__ __forceinline__ float rscat(const float (&s)[8], int lane) {
  float t0 = s32(s[0], s[4]), t1 = s32(s[1], s[5]), t2 = s32(s[2], s[6]), t3 = s32(s[3], s[7]);
  float u0 = s16(t0, t2), u1 = s16(t1, t3);
  const bool b3 = lane & 8;
  return (b3 ? u1 : u0) + dpp<ROR8>(b3 ? u0 : u1);
}
__device__ __forceinline__ void ln_part(float x, int w, int lane) {
  const float x0 = asf(__builtin_amdgcn_readfirstlane(asu(x)));
  const float d = x - x0;
  const float s1 = wsum(d), s2 = wsum(d * d);
  if (lane == 0) sts<v2f>(L_LN + w * 8, v2f{x0 + s1 * (1.f / 64), s2 - s1 * s1 * (1.f / 64)});
}
__device__ __forceinline__ void ln_fin(float& mean, float& rstd) {
  const v4f a = lds<v4f>(L_LN), b = lds<v4f>(L_LN + 16), c = lds<v4f>(L_LN + 32);
  const v2f d = lds<v2f>(L_LN + 48);
  mean = (((a.x + a.z) + (b.x + b.z)) + ((c.x + c.z) + d.x)) * (1.f / 7);
  const float e0 = a.x - mean, e1 = a.z - mean, e2 = b.x - mean, e3 = b.z - mean, e4 = c.x - mean, e5 = c.z - mean, e6 = d.x - mean;
  const float m2 = (((a.y + a.w) + (b.y + b.w)) + ((c.y + c.w) + d.y)) + 64.f * ((e0 * e0 + e1 * e1 + e2 * e2 + e3 * e3) + (e4 * e4 + e5 * e5 + e6 * e6));
  rstd = __builtin_amdgcn_rsqf(m2 * (1.f / 448) + 1e-5f);
}
__device__ __forceinline__ float pick(float a, float b, float c, float d, int q) {
  const float lo = (q & 1) ? b : a, hi = (q & 1) ? d : c;
  return (q & 2) ? hi : lo;
}
__device__ __forceinline__ u32 wtr(u32 base, int lane) {
  const int G = lane >> 4, r = (lane >> 2) & 3, p = lane & 3;
  return base + (8 * G + r) * 128 + ((((r >> 1) & 1) | ((G & 1) << 1)) << 5) + p * 8;
}
__device__ __forceinline__ v4u wfrag(u32 a, int ks, int ct) { const u32 b = (a ^ (ct << 5)) + ks * 4096; return tr2(b, b + 512); }
__device__ __forceinline__ u32 wsrc(int lane, int odd) { return (u32)(((lane & 7) ^ ((((lane >> 4) & 1) | (odd << 1)) << 1)) << 4); }

#define TS(i)
#define TSL(i, j)
#define TSOUT()
#define NPRE 4
#define LD16(o) __builtin_amdgcn_raw_buffer_load_b128(rw, (o), 0, SC1)

__device__ __forceinline__ float gather_c(rsrc_t rw, u32 base, int lane) {
  const u32 o = base + lane * 16, o3 = lane < 40 ? o + 3072 : OOB;
  v4u r0 = LD16(o), r1 = LD16(o + 1024), r2 = LD16(o + 2048), r3 = LD16(o3);
  for (int sp = 0; sp < kSpin; ++sp) {
    const bool bad = max(max(mx4(r0), mx4(r1)), max(mx4(r2), mx4(r3))) == SENT;
    if (!__any(bad)) break;
    nap<D_RT>();
    asm volatile("" ::: "memory");
    if (bad) { r0 = LD16(o); r1 = LD16(o + 1024); r2 = LD16(o + 2048); r3 = LD16(o3); }
  }
  float s[8] = {0.f, 0.f, 0.f, 0.f, 0.f, 0.f, 0.f, 0.f};
  acc8(s, r0); acc8(s, r1); acc8(s, r2); acc8(s, r3);
  return rscat(s, lane);
}

__global__ void k_pre(char* ws) { __builtin_amdgcn_raw_buffer_store_b32((u32)__builtin_amdgcn_s_memrealtime(), mk(ws + (3u << 20), 1 << 17), threadIdx.x == 0 ? 65536 + blockIdx.x * 4 : OOB, 0, SC1); }
__global__ void __launch_bounds__(TNT) k_test(char* ws) { __builtin_amdgcn_raw_buffer_store_b32((u32)__builtin_amdgcn_s_memrealtime(), mk(ws + (3u << 20), 1 << 17), threadIdx.x == 0 ? blockIdx.x * 4 : OOB, 0, SC1);
  if (TVG) asm volatile("v_mov_b32 v160, 0" ::: "v160"); }
__global__ void k_post(char* ws) {
 const u32 tz = (u32)__builtin_amdgcn_s_memrealtime();
 u32 mn = ~0u, mx = 0, ta = 0;
 for (int i = 0; i < TNB; ++i) { u32 t = ((u32*)(ws + (3u << 20)))[i]; mn = min(mn, t); mx = max(mx, t); }
 for (int i = 0; i < PNB; ++i) { u32 t = ((u32*)(ws + (3u << 20) + 65536))[i]; ta = max(ta, t); }
 const u32 dl = PI == 0 ? 0u : PI == 1 ? 4 * (mn - ta) : 4 * (mx - ta);
 while ((u32)__builtin_amdgcn_s_memrealtime() - tz < min(dl, 20000u) + 1000u) {} }
__global__ void __launch_bounds__(768, 3)
fused_kernel(const unsigned short* __restrict__ xin, const char* __restrict__ Kc, const char* __restrict__ Vc, const char* __restrict__ wq,
             const char* __restrict__ wo, const char* __restrict__ w1, const char* __restrict__ w2, unsigned short* __restrict__ out,
             char* __restrict__ ws) {
  TS(0);
  const int lane = threadIdx.x & 63, w = __builtin_amdgcn_readfirstlane(threadIdx.x >> 6);
  const int g = blockIdx.x & 7, m = blockIdx.x >> 3;
  const int n = lane & 15, q = lane >> 4;
  const bool is_o = m < 14, is_f = m >= 28 && (m - 28) * 8 + g < 28;
  const rsrc_t rw = mk(ws, WS_END);
  const v4f Z = {0.f, 0.f, 0.f, 0.f};

  if (w < 4) {
    const int a = w;
    v4u kf[16], vf[16];
    {
      const size_t cu = ((size_t)g * kSK + (kSK - kW) + m * 128) * 512;
      const rsrc_t rk = mk(Kc + cu, 65536), rv = mk(Vc + cu, 65536);
      const int hi = lane >> 5, p = lane & 31;
      const u32 vk = hi * 512 + ((p ^ hi) << 4), vv = hi * 512 + ((p ^ (hi << 1)) << 4);
#pragma unroll
      for (int i = 0; i < 16; ++i) dma_nt(L_K + a * 16384 + i * 1024, rk, vk ^ ((2 * i & 15) << 4), (((i + m) & 15) * 4 + a) * 1024);
#pragma unroll
      for (int i = 0; i < 16; ++i) dma_nt(L_V + a * 16384 + i * 1024, rv, vv ^ ((2 * i & 7) << 5), (((i + m) & 15) * 4 + a) * 1024);
      TS(1);
      WAITV(16);
      const u32 ka = L_K + a * 16384 + n * 512 + ((q ^ n) << 4);
#pragma unroll
      for (int i = 0; i < 16; ++i) kf[i] = lds<v4u>((ka ^ ((i & 3) << 6)) + ((i >> 2) & 1) * 256 + (i >> 3) * 8192);
      TS(2);
    }
#pragma unroll
    for (int L = 0; L < 2; ++L) {
      BAR();
      TSL(5, 15);
      v4f sc0 = Z, sc1 = Z;
#pragma unroll
      for (int i = 0; i < 8; ++i) {
        const v4u b = lds<v4u>(L_QS + (n & 1) * 576 + i * 64 + q * 16);
        sc0 = mfma(kf[i], b, sc0);
        sc1 = mfma(kf[8 + i], b, sc1);
      }
      constexpr float c = 1.4426950408889634f / 16;
      float mx = fmaxf(fmaxf(fmaxf(sc0.x, sc0.y), fmaxf(sc0.z, sc0.w)), fmaxf(fmaxf(sc1.x, sc1.y), fmaxf(sc1.z, sc1.w)));
      mx = x32(x16(mx, true), true) * c;
      v4f p0, p1;
#pragma unroll
      for (int j = 0; j < 4; ++j) { p0[j] = __builtin_amdgcn_exp2f(sc0[j] * c - mx); p1[j] = __builtin_amdgcn_exp2f(sc1[j] * c - mx); }
      const v4u P = {pk(p0.x, p0.y), pk(p0.z, p0.w), pk(p1.x, p1.y), pk(p1.z, p1.w)};
      const float l = x32(x16(((p0.x + p0.y) + (p0.z + p0.w)) + ((p1.x + p1.y) + (p1.z + p1.w)), false), false);
      if (lane < 2) { sts<float>(L_ST + lane * 32 + a * 4, mx); sts<float>(L_ST + lane * 32 + 16 + a * 4, l); }
      if (L == 0) {
        const int r = (lane >> 2) & 3, p = lane & 3;
        const u32 va = L_V + a * 16384 + (4 * q + r) * 512 + ((4 * (q & 1) + r) << 5) + p * 8;
        TS(6);
        WAITV(8);
#pragma unroll
        for (int i = 0; i < 16; ++i) { const v2u x = trd((va ^ ((i & 7) << 5)) + (i >> 3) * 256); vf[i].x = x.x; vf[i].y = x.y; }
        WAITV(0);
        TS(7);
#pragma unroll
        for (int t = 0; t < 8; ++t)
#pragma unroll
          for (int T = 0; T < 2; ++T) { const v2u x = trd((va ^ (t << 5)) + T * 256 + 8192); vf[8 * T + t].z = x.x; vf[8 * T + t].w = x.y; }
      }
      v4f o0 = Z, o1 = Z;
#pragma unroll
      for (int t = 0; t < 8; ++t) {
        const bool on = (n >> 1) == t;
        const v4u A = {on ? P.x : 0u, on ? P.y : 0u, on ? P.z : 0u, on ? P.w : 0u};
        o0 = mfma(A, vf[t], o0);
        o1 = mfma(A, vf[8 + t], o1);
      }
      const u32 oa = L_OP + a * 2048 + (32 * q + n) * 4;
#pragma unroll
      for (int r = 0; r < 4; ++r) {
        sts<float>(oa + (r & 1) * 1024 + (r >> 1) * 64, o0[r]);
        sts<float>(oa + (r & 1) * 1024 + (r >> 1) * 64 + 512, o1[r]);
      }
      BAR();
      TSL(8, 16);
      {
        const int h = a >> 1, e = 64 * (a & 1) + lane;
        const v4f ms = lds<v4f>(L_ST + h * 32), ls = lds<v4f>(L_ST + h * 32 + 16);
        v2f ov[4];
#pragma unroll
        for (int j = 0; j < 4; ++j) ov[j] = lds<v2f>(L_OP + j * 2048 + h * 1024 + e * 8);
        const float M = fmaxf(fmaxf(ms.x, ms.y), fmaxf(ms.z, ms.w));
        v4f fa;
#pragma unroll
        for (int j = 0; j < 4; ++j) fa[j] = __builtin_amdgcn_exp2f(ms[j] - M);
        const float Ls = (fa.x * ls.x + fa.y * ls.y) + (fa.z * ls.z + fa.w * ls.w);
        const float inv = __builtin_amdgcn_rcpf(Ls);
        const float y0 = ((fa.x * ov[0].x + fa.y * ov[1].x) + (fa.z * ov[2].x + fa.w * ov[3].x)) * inv;
        const float y1 = ((fa.x * ov[0].y + fa.y * ov[1].y) + (fa.z * ov[2].y + fa.w * ov[3].y)) * inv;
        __builtin_amdgcn_raw_buffer_store_b32(min(pk(y0, y1), SENT - 1), rw,
            WS_A + h * 16384 + (e >> 5) * 4096 + m * 128 + (((((e >> 2) & 7) ^ ((m >> 1) & 7)) << 4) | ((e & 3) << 2)), (L * 8 + g) * 32768, SC1);
        if (!(a & 1))
          __builtin_amdgcn_raw_buffer_store_b32(min(asu(M + __builtin_amdgcn_logf(Ls)), SENT - 1), rw,
              lane < 32 ? WS_S + h * 4096 + m * 128 + lane * 4 : OOB, (L * 8 + g) * 8192, SC1);
      }
      TSL(9, 17);
      if (L) break;
      if (is_o || is_f) { BAR(); BAR(); }
      BAR();
    }
    TSOUT();
    return;
  }

  const int v = w - 4;
  const int f = (m - 28) * 8 + g, hh = m & 1, cb = m >> 1;
  const v4u ONES = {0x3f803f80u, 0x3f803f80u, 0x3f803f80u, 0x3f803f80u};
  v4u wf[16];
  v4u qf2[2];
  float csq = 0.f, cs1 = 0.f, xres = 0.f;
  __builtin_amdgcn_s_setprio(3);
  const u32 tag = *(const u32*)ws * 0x9e3779b5u + 0x7f4a7c15u;
  if (v == 0) {
    const v4u S = {SENT, SENT, SENT, SENT};
    const u32 l8 = lane >> 3, c16 = (lane & 7) * 16;
    __builtin_amdgcn_raw_buffer_store_b128(S, rw, WS_A + l8 * 4096 + m * 128 + c16, g * 32768, SC1);
    __builtin_amdgcn_raw_buffer_store_b128(S, rw, WS_A + l8 * 4096 + m * 128 + c16, (8 + g) * 32768, SC1);
    u32 o3 = OOB;
    if (l8 < 4) o3 = WS_S + ((l8 >> 1) * 8 + g) * 8192 + (l8 & 1) * 4096 + m * 128 + c16;
    else if (l8 < 6 && is_o) o3 = WS_B + ((l8 & 1) * 7 + cb) * 2048 + (2 * g + hh) * 128 + c16;
    __builtin_amdgcn_raw_buffer_store_b128(S, rw, o3, 0, SC1);
    if (is_f) {
      const u32 o4 = l8 < 7 ? WS_C + l8 * 4096 + f * 128 + c16 : OOB;
      __builtin_amdgcn_raw_buffer_store_b128(S, rw, o4, 0, SC1);
      __builtin_amdgcn_raw_buffer_store_b128(S, rw, o4, 7 * 4096, SC1);
      if (f == 0) {
        __builtin_amdgcn_raw_buffer_store_b128(S, rw, o4, 28 * 128, SC1);
        __builtin_amdgcn_raw_buffer_store_b128(S, rw, o4, 7 * 4096 + 28 * 128, SC1);
      }
    }
    v4u xv;
    asm volatile("buffer_load_dwordx4 %0, %1, %2, 0 offen" : "=v"(xv) : "v"(lane * 16), "s"(mk(xin, 896)) : "memory");
    const rsrc_t rq = mk(wq, 448 * 8192);
    const u32 vq = (lane >> 1) * 8192 + (g * 512 + m * 16) * 2 + (lane & 1) * 16;
#pragma unroll
    for (int i = 0; i < 14; ++i) dma_nt(L_WQ + i * 1024, rq, vq, i * 32 * 8192);
    TS(1);
    asm volatile("s_waitcnt vmcnt(14)" : "+v"(xv)::"memory");
    float xs[8] = {0.f, 0.f, 0.f, 0.f, 0.f, 0.f, 0.f, 0.f};
    acc8(xs, xv);
    if (lane < 56) sts<v4u>(L_XS + lane * 16, xv);
    const float mean = wsum(((xs[0] + xs[1]) + (xs[2] + xs[3])) + ((xs[4] + xs[5]) + (xs[6] + xs[7]))) * (1.f / 448);
    float m2 = 0.f;
#pragma unroll
    for (int j = 0; j < 8; ++j) m2 += (xs[j] - mean) * (xs[j] - mean);
    const float rstd = __builtin_amdgcn_rsqf(wsum(lane < 56 ? m2 : 0.f) * (1.f / 448) + 1e-5f);
    WAITV(0);
    TS(2);
    v4f aq = Z, as = Z;
    const u32 qa = L_WQ + (8 * q + ((lane >> 2) & 3)) * 32 + (lane & 3) * 8;
#pragma unroll
    for (int i = 0; i < 14; ++i) {
      const v4u b = tr2(qa + i * 1024, qa + i * 1024 + 128);
      aq = mfma(lds<v4u>(L_XS + i * 64 + q * 16), b, aq);
      as = mfma(ONES, b, as);
    }
    csq = as.x;
    const v2u gq = {asu(rbf((aq.x - mean * csq) * rstd)), tag};
    __builtin_amdgcn_raw_buffer_store_b64(gq, rw, lane < 16 ? WS_Q + (g * 32 + m) * 128 + lane * 8 : OOB, 0, SC1);
    TS(3);
    nap<D_Q0 / 3>();
  } else if (v < 4) {
    nap<D_Q0>();
  }

#pragma unroll
  for (int L = 0; L < 2; ++L) {
    if (v < 4) {
      const u32 o = WS_Q + (L * 8 + g) * 4096 + v * 1024 + lane * 16, of = L ? o : WS_Q + (v * 64 + lane) * 128;
      v4u r = LD16(o), fl = r;
      if (L == 0) fl = LD16(of);
      for (int sp = 0; sp < kSpin; ++sp) {
        const bool bad = r.y != tag || r.w != tag || fl.y != tag || fl.w != tag;
        if (!__any(bad)) break;
        nap<D_RT>();
        asm volatile("" ::: "memory");
        if (bad) { r = LD16(o); fl = r; if (L == 0) fl = LD16(of); }
      }
      sts<u32>(L_QS + (v >> 1) * 576 + (v & 1) * 256 + lane * 4, (r.x >> 16) | (r.z & 0xffff0000u));
    }
    TSL(4, 14);
    BAR();
    TSL(5, 15);
    if (L == 0) {
      if (blockIdx.x == 0 && v == 1) __builtin_amdgcn_raw_buffer_store_b32(tag, rw, lane == 0 ? 0 : OOB, 0, SC1);
      __builtin_amdgcn_s_setprio(0);
      if (is_o) {
        const rsrc_t ro = mk(wo, 4096 * 896);
#pragma unroll
        for (int i = 0; i < 4; ++i)
          dma_nt(L_K + (v * 4 + i) * 1024, ro, (lane >> 3) * 896 + wsrc(lane, i & 1), ((2 * g + hh) * 256 + (v * 4 + i) * 8) * 896 + cb * 128);
      } else if (is_f && v < 7) {
        const rsrc_t r1 = mk(w1, 448 * 3584);
#pragma unroll
        for (int i = 0; i < NPRE; ++i)
          dma_nt(L_K + (v * 8 + i) * 1024, r1, (lane >> 3) * 3584 + wsrc(lane, i & 1), (v * 8 + i) * 8 * 3584 + f * 128);
      }
      if (v < 7) {
        const u32 qa = L_WQ + v * 2048 + (8 * q + ((lane >> 2) & 3)) * 32 + (lane & 3) * 8;
        qf2[0] = tr2(qa, qa + 128);
        qf2[1] = tr2(qa + 1024, qa + 1024 + 128);
      }
    }
    BAR();
    TSL(8, 16);
    if (is_o) {
      const int kp = v >> 1, st = v & 1;
      const u32 sa = WS_A + (L * 8 + g) * 32768 + hh * 16384 + kp * 4096 + st * 2048;
      const u32 so = WS_S + (L * 8 + g) * 8192 + hh * 4096 + (16 * st + 4 * q + (lane & 3)) * 128;
      const u32 stg = L_V + v * 2048, aa = stg + n * 128 + ((q ^ (n >> 1)) << 4);
      if (L == 0) {
        WAITV(0);
        BAR();
        const u32 wa = wtr(L_K + kp * 8192, lane);
#pragma unroll
        for (int i = 0; i < 8; ++i) wf[i] = wfrag(wa, i >> 2, i & 3);
      }
      nap<D_A>();
      v4u a0, a1;
      u32 lse;
      for (int sp = 0; sp < kSpin; ++sp) {
        dma_sc1(stg, rw, lane * 16, sa);
        dma_sc1(stg + 1024, rw, lane * 16, sa + 1024);
        lse = __builtin_amdgcn_raw_buffer_load_b32(rw, so, 0, SC1);
        WAITV(0);
        a0 = lds<v4u>(aa); a1 = lds<v4u>(aa ^ 64);
        if (!__any(max(max(mx4(a0), mx4(a1)), lse) == SENT)) break;
        nap<D_RT>();
      }
      TSL(10, 18);
      v4f y[4];
#pragma unroll
      for (int c = 0; c < 4; ++c) y[c] = mfma(a1, wf[4 + c], mfma(a0, wf[c], Z));
      float M = asf(lse);
      M = fmaxf(M, dpp<QX1>(M)); M = fmaxf(M, dpp<QX2>(M));
      M = x32(x16(M, true), true);
      const float e = __builtin_amdgcn_exp2f(asf(lse) - M);
      float Ls = e + dpp<QX1>(e);
      Ls += dpp<QX2>(Ls);
      Ls = x32(x16(Ls, false), false);
      const float e0 = dpp<0x00>(e), e1 = dpp<0x55>(e), e2 = dpp<0xaa>(e), e3 = dpp<0xff>(e);
      float z[4];
#pragma unroll
      for (int c = 0; c < 4; ++c) z[c] = (e0 * y[c].x + e1 * y[c].y) + (e2 * y[c].z + e3 * y[c].w);
      const float zz = s16(s32(z[0], z[2]), s32(z[1], z[3]));
      sts<float>(L_RED + v * 256 + lane * 4, zz);
      if (lane == 0 && kp == 0) sts<v2f>(L_LN + st * 8, v2f{M, Ls});
      BAR();
      if (v == 0) {
        const v4f sl = lds<v4f>(L_LN);
        float r0 = 0.f, r1 = 0.f;
#pragma unroll
        for (int j = 0; j < 4; ++j) { r0 += lds<float>(L_RED + j * 512 + lane * 4); r1 += lds<float>(L_RED + j * 512 + 256 + lane * 4); }
        const float Mx = fmaxf(sl.x, sl.z), f0 = __builtin_amdgcn_exp2f(sl.x - Mx), f1 = __builtin_amdgcn_exp2f(sl.z - Mx);
        const float y = (f0 * r0 + f1 * r1) * __builtin_amdgcn_rcpf(f0 * sl.y + f1 * sl.w);
        __builtin_amdgcn_raw_buffer_store_b32(min(pk(y, dpp<QX1>(y)), SENT - 1), rw,
            (lane & 1) ? OOB : WS_B + (L * 7 + cb) * 2048 + (2 * g + hh) * 128 + lane * 2, 0, SC1);
      }
      TSL(11, 19);
    } else if (is_f) {
      if (L == 0) {
        if (v < 7) {
          nap<D_TAIL>();
          const rsrc_t r1 = mk(w1, 448 * 3584), r2 = mk(w2, 1792 * 896);
#pragma unroll
          for (int i = NPRE; i < 8; ++i)
            dma_nt(L_K + (v * 8 + i) * 1024, r1, (lane >> 3) * 3584 + wsrc(lane, i & 1), (v * 8 + i) * 8 * 3584 + f * 128);
#pragma unroll
          for (int i = 0; i < 8; ++i)
            dma_nt(L_V + (v * 8 + i) * 1024, r2, (lane >> 3) * 896 + wsrc(lane, i & 1), (f * 64 + i * 8) * 896 + v * 128);
          xres = asf((u32)xin[v * 64 + (lane & 7) * 8 + (lane >> 3)] << 16);
          WAITV(0);
          const u32 wa = wtr(L_K + v * 8192, lane), wb = wtr(L_V + v * 8192, lane);
#pragma unroll
          for (int i = 0; i < 8; ++i) { wf[i] = wfrag(wa, i >> 2, i & 3); wf[8 + i] = wfrag(wb, i >> 2, i & 3); }
          v4f cs[4];
#pragma unroll
          for (int c = 0; c < 4; ++c) cs[c] = mfma(ONES, wf[4 + c], mfma(ONES, wf[c], Z));
          sts<float>(L_CS + v * 256 + lane * 4, pick(cs[0].x, cs[1].x, cs[2].x, cs[3].x, q));
        }
        BAR();
#pragma unroll
        for (int j = 0; j < 7; ++j) cs1 += lds<float>(L_CS + j * 256 + lane * 4);
        TS(9);
      } else {
        nap<D_B>();
      }
      if (v < 7) {
        const u32 o = WS_B + (L * 7 + v) * 2048 + lane * 16;
        v4u r0 = LD16(o), r1 = LD16(o + 1024);
        for (int sp = 0; sp < kSpin; ++sp) {
          const bool bad = max(mx4(r0), mx4(r1)) == SENT;
          if (!__any(bad)) break;
          nap<D_RT>();
          asm volatile("" ::: "memory");
          if (bad) { r0 = LD16(o); r1 = LD16(o + 1024); }
        }
        TSL(10, 18);
        float s[8] = {0.f, 0.f, 0.f, 0.f, 0.f, 0.f, 0.f, 0.f};
        acc8(s, r0); acc8(s, r1);
        const float xs = xres + rscat(s, lane);
        const u32 xb = pk(xs, xs);
        sts<unsigned short>(L_XS + v * 128 + ((lane & 7) * 8 + (lane >> 3)) * 2, (unsigned short)xb);
        asm volatile("" ::: "memory");
        const v4u a0 = lds<v4u>(L_XS + v * 128 + q * 16), a1 = lds<v4u>(L_XS + v * 128 + 64 + q * 16);
        ln_part(asf(xb << 16), v, lane);
        v4f y[4];
#pragma unroll
        for (int c = 0; c < 4; ++c) y[c] = mfma(a1, wf[4 + c], mfma(a0, wf[c], Z));
        sts<float>(L_RED + v * 256 + lane * 4, pick(y[0].x, y[1].x, y[2].x, y[3].x, q));
        if (f == 0)
          __builtin_amdgcn_raw_buffer_store_b32(min(lds<u32>(L_XS + v * 128 + (lane & 31) * 4), SENT - 1), rw,
              lane < 32 ? WS_C + (L * 7 + v) * 4096 + 28 * 128 + lane * 4 : OOB, 0, SC1);
      }
      BAR();
      if (v < 7) {
        float mean, rstd, r = 0.f;
        ln_fin(mean, rstd);
#pragma unroll
        for (int j = 0; j < 7; ++j) r += lds<float>(L_RED + j * 256 + lane * 4);
        const float av = rbf((r - mean * cs1) * rstd);
        const float hv = av * __builtin_amdgcn_rcpf(1.f + __builtin_amdgcn_exp2f(av * -1.4426950408889634f));
        sts<unsigned short>(L_HS + v * 128 + lane * 2, (unsigned short)pk(hv, hv));
        asm volatile("" ::: "memory");
        const v4u a0 = lds<v4u>(L_HS + v * 128 + q * 16), a1 = lds<v4u>(L_HS + v * 128 + 64 + q * 16);
        v4f y[4];
#pragma unroll
        for (int c = 0; c < 4; ++c) y[c] = mfma(a1, wf[12 + c], mfma(a0, wf[8 + c], Z));
        const float yv = pick(y[0].x, y[1].x, y[2].x, y[3].x, q);
        __builtin_amdgcn_raw_buffer_store_b32(min(pk(yv, dpp<QX1>(yv)), SENT - 1), rw,
            (lane & 1) ? OOB : WS_C + (L * 7 + v) * 4096 + f * 128 + lane * 2, 0, SC1);
      }
      TSL(11, 19);
    }
    if (L && !(m == 20 && v == g)) break;
    if (!is_f) nap<D_C>();
    if (v < 7) {
      const float xs = gather_c(rw, WS_C + (L * 7 + v) * 4096, lane);
      const u32 xb = pk(xs, xs);
      TSL(12, 20);
      if (L) { out[v * 64 + (lane & 7) * 8 + (lane >> 3)] = (unsigned short)xb; break; }
      xres = asf(xb << 16);
      sts<unsigned short>(L_XS + v * 128 + ((lane & 7) * 8 + (lane >> 3)) * 2, (unsigned short)xb);
      asm volatile("" ::: "memory");
      const v4u a0 = lds<v4u>(L_XS + v * 128 + q * 16), a1 = lds<v4u>(L_XS + v * 128 + 64 + q * 16);
      ln_part(xres, v, lane);
      const v4f aq = mfma(a1, qf2[1], mfma(a0, qf2[0], Z));
      if (lane < 16) sts<float>(L_RED + v * 256 + lane * 4, aq.x);
    }
    if (L) break;
    BAR();
    if (v == 0) {
      float mean, rstd, r = 0.f;
      ln_fin(mean, rstd);
#pragma unroll
      for (int j = 0; j < 7; ++j) r += lds<float>(L_RED + j * 256 + n * 4);
      const v2u gq = {asu(rbf((r - mean * csq) * rstd)), tag};
      __builtin_amdgcn_raw_buffer_store_b64(gq, rw, lane < 16 ? WS_Q + ((8 + g) * 32 + m) * 128 + lane * 8 : OOB, 0, SC1);
    }
    TS(13);
    if (v < 4) nap<D_Q1>();
  }
  TSOUT();
}
}

extern "C" void kernel_launch(void* const* d_in, const int* in_sizes, int n_in, void* d_out, int out_size, void* d_ws, size_t ws_size,
                              hipStream_t stream) {
  (void)hipFuncSetAttribute((const void*)fused_kernel, hipFuncAttributeMaxDynamicSharedMemorySize, L_END);
  (void)hipFuncSetAttribute((const void*)k_test, hipFuncAttributeMaxDynamicSharedMemorySize, 163840);
  k_pre<<<PNB, 64, 0, stream>>>((char*)d_ws);
  k_test<<<TNB, TNT, TLDS, stream>>>((char*)d_ws);
  fused_kernel<<<256, 768, L_END, stream>>>((const unsigned short*)d_in[0], (const char*)d_in[1], (const char*)d_in[2], (const char*)d_in[3],
                                            (const char*)d_in[4], (const char*)d_in[6], (const char*)d_in[7], (unsigned short*)d_out, (char*)d_ws);
  k_post<<<1, 64, 0, stream>>>((char*)d_ws);
}
```

```cpp
#include <hip/hip_runtime.h>

namespace {
__global__ void __launch_bounds__(768, 3)
fused_kernel(const void* x, const void* k, const void* v, const void* wq, const void* wo, const void* w1, const void* w2, void* out, void* ws) {
#if defined(__HIP_DEVICE_COMPILE__)
  asm volatile(
      "s_load_dwordx16 s[4:19], s[0:1], 0x0\n"
      "s_load_dwordx2 s[20:21], s[0:1], 0x40\n"
      "v_readfirstlane_b32 s22, v0\n"
      "v_and_b32 v0, 63, v0\n"
      "s_and_b32 s36, s2, 7\n"
      "s_lshr_b32 s37, s2, 3\n"
      "s_lshr_b32 s22, s22, 6\n"
      "s_cmp_lt_u32 s22, 5\n"
      "s_cbranch_scc1 .Lfkpret\n"
      "s_mov_b64 exec, 0\n"
      "s_cmp_eq_u32 s22, 5\n"
      "s_cbranch_scc1 .Lfkpq0\n"
      "s_cmp_eq_u32 s22, 6\n"
      "s_cbranch_scc1 .Lfkpq1\n"
      "s_cmp_eq_u32 s22, 7\n"
      "s_cbranch_scc1 .Lfkpa0\n"
      "s_cmp_eq_u32 s22, 8\n"
      "s_cbranch_scc1 .Lfkpa0b\n"
      "s_cmp_eq_u32 s22, 9\n"
      "s_cbranch_scc1 .Lfkpa0c\n"
      "s_cmp_eq_u32 s22, 10\n"
      "s_cbranch_scc1 .Lfkpa0d\n"
      "s_cmp_eq_u32 s22, 11\n"
      "s_cbranch_scc1 .Lfkpp0\n"
      ".Lfkpret:\n"
      "s_mov_b64 exec, -1\n"
      "s_mul_i32 s38, s36, 115712\n"
      "s_add_u32 s38, s38, 151552\n"
      "v_lshlrev_b32 v1, 4, v0\n"
      "s_mov_b32 s26, 1150976\n"
      "s_mov_b32 s27, 0x20000\n"
      "s_lshr_b32 s94, s37, 4\n"
      "s_add_u32 s94, s94, 1\n"
      "s_and_b32 s40, s37, 3\n"
      "s_cmp_eq_u32 s40, 3\n"
      "s_cselect_b32 s94, s94, 0\n"
      "s_cmp_eq_u32 s22, 4\n"
      "s_waitcnt lgkmcnt(0)\n"
      "s_load_dword s23, s[20:21], 0x0\n"
      "s_mov_b32 s24, s20\n"
      "s_cbranch_scc1 .Lfk_qw1\n"
      "s_and_b32 s25, s21, 0xffff\n"
      "s_cmp_gt_u32 s22, 4\n"
      "s_cbranch_scc1 .Lfk_proj2\n"
      "s_lshl_b32 s40, s36, 16\n"
      "s_lshl_b32 s41, s37, 7\n"
      "s_add_u32 s40, s40, s41\n"
      "s_add_u32 s40, s40, 0xf000\n"
      "s_lshr_b32 s41, s40, 23\n"
      "s_lshl_b32 s40, s40, 9\n"
      "s_add_u32 s28, s6, s40\n"
      "s_addc_u32 s29, s7, s41\n"
      "s_and_b32 s29, s29, 0xffff\n"
      "s_mov_b32 s30, 0x10000\n"
      "s_mov_b32 s31, 0x20000\n"
      "s_add_u32 s32, s8, s40\n"
      "s_addc_u32 s33, s9, s41\n"
      "s_and_b32 s33, s33, 0xffff\n"
      "s_mov_b32 s34, 0x10000\n"
      "s_mov_b32 s35, 0x20000\n"
      "v_lshrrev_b32 v2, 5, v0\n"
      "v_and_b32 v3, 31, v0\n"
      "v_xor_b32 v4, v3, v2\n"
      "v_lshlrev_b32 v5, 1, v2\n"
      "v_xor_b32 v5, v3, v5\n"
      "v_lshlrev_b32 v2, 9, v2\n"
      "v_lshl_or_b32 v4, v4, 4, v2\n"
      "v_lshl_or_b32 v5, v5, 4, v2\n"
      "s_lshl_b32 s42, s22, 14\n"
      "s_add_u32 m0, s42, 32768\n"
      "s_lshl_b32 s43, s37, 12\n"
      "s_nop 0\n"
      "s_lshl_b32 s44, s22, 10\n"
      "s_mov_b32 s45, 0\n"
      "s_lshl_b32 s47, s22, 11\n"
      "s_add_u32 s48, s47, 2048\n"
      "s_add_u32 s47, s47, s38\n"
      "s_add_u32 s47, s47, 81920\n"
      "s_sub_u32 s82, s37, 19\n"
      "s_and_b32 s40, s82, 3\n"
      "s_cselect_b32 s82, 64, s82\n"
      "s_lshl_b32 s82, s82, 1\n"
      "s_add_u32 s82, s82, s36\n"
      "s_cmp_gt_u32 s82, 31\n"
      "s_cselect_b32 s86, 1, 0\n"
      "s_cselect_b32 s87, 4, 4\n"
      "s_cmp_lt_u32 s82, 28\n"
      "s_cselect_b32 s86, 2, s86\n"
      "s_cmp_eq_u32 s94, 2\n"
      "s_cselect_b32 s87, 11, s87\n"
      "s_lshl_b32 s87, s87, 10\n"
      "s_cmp_eq_u32 s94, 2\n"
      "s_cbranch_scc1 .Lfk_dk4\n"
      "s_sleep 26\n"
      ".Lfk_dk4:\n"
      ".Lfk_k5:\n"
      "s_and_b32 s46, s43, 0xf000\n"
      "s_or_b32 s46, s46, s44\n"
      "v_xor_b32 v6, s45, v4\n"
      "s_add_u32 s45, s45, 32\n"
      "s_and_b32 s45, s45, 0xe0\n"
      "buffer_load_dwordx4 v6, s[28:31], s46 offen nt lds\n"
      "s_add_u32 s43, s43, 0x1000\n"
      "s_add_u32 m0, m0, 0x400\n"
      "s_and_b32 s50, m0, 0x3c00\n"
      "s_cbranch_scc1 .Lfk_k5\n"
      "s_add_u32 m0, s42, 98304\n"
      ".Lfk_v6:\n"
      "s_and_b32 s50, m0, 0x3c00\n"
      "s_cmp_lg_u32 s50, s87\n"
      "s_cbranch_scc1 .Lfk_v7\n"
      "s_mov_b32 s51, m0\n"
      "s_mov_b32 m0, s48\n"
      "s_nop 0\n"
      "buffer_load_dwordx4 v1, s[24:27], s47 offen sc1 lds\n"
      "buffer_load_dwordx4 v1, s[24:27], s47 offen offset:1024 sc1 lds\n"
      "s_mov_b32 m0, s51\n"
      ".Lfk_v7:\n"
      "s_and_b32 s46, s43, 0xf000\n"
      "s_or_b32 s46, s46, s44\n"
      "v_xor_b32 v6, s45, v5\n"
      "s_add_u32 s45, s45, 64\n"
      "s_and_b32 s45, s45, 0xc0\n"
      "buffer_load_dwordx4 v6, s[32:35], s46 offen nt lds\n"
      "s_add_u32 s43, s43, 0x1000\n"
      "s_add_u32 m0, m0, 0x400\n"
      "s_and_b32 s50, m0, 0x3c00\n"
      "s_cbranch_scc1 .Lfk_v6\n"
      "s_branch .Lfk_att3\n"
      ".Lfk_qw1:\n"
      "s_setprio 3\n"
      "s_and_b32 s25, s21, 0xffff\n"
      "s_cmp_eq_u32 s94, 2\n"
      "s_cbranch_scc1 .Lfk_qn9\n"
      "s_mov_b32 s28, s4\n"
      "s_and_b32 s29, s5, 0xffff\n"
      "s_mov_b32 s30, 896\n"
      "s_mov_b32 s31, 0x20000\n"
      "s_mov_b32 m0, 10240\n"
      "s_mov_b32 s32, s10\n"
      "s_and_b32 s33, s11, 0xffff\n"
      "s_mov_b32 s34, 3670016\n"
      "s_mov_b32 s35, 0x20000\n"
      "buffer_load_dwordx4 v1, s[28:31], 0 offen lds\n"
      "v_lshrrev_b32 v2, 3, v0\n"
      "v_and_b32 v3, 2, v2\n"
      "v_and_b32 v4, 7, v0\n"
      "v_xor_b32 v3, v3, v4\n"
      "v_lshlrev_b32 v3, 4, v3\n"
      "v_lshl_or_b32 v2, v2, 13, v3\n"
      "s_and_b32 s40, s37, 3\n"
      "s_lshl_b32 s41, s40, 20\n"
      "v_add_u32 v2, s41, v2\n"
      "v_xor_b32 v3, 64, v2\n"
      "v_add_u32 v3, 0x10000, v3\n"
      "s_lshr_b32 s41, s37, 2\n"
      "s_lshl_b32 s42, s36, 3\n"
      "s_add_u32 s41, s41, s42\n"
      "s_lshl_b32 s41, s41, 7\n"
      "s_mov_b32 m0, 16384\n"
      ".Lfk_wq10:\n"
      "s_cmp_lg_u32 m0, 24576\n"
      "s_cbranch_scc1 .Lfk_wq11\n"
      "s_cmp_eq_u32 s94, 1\n"
      "s_cbranch_scc0 .Lfk_wq11\n"
      "v_subrev_u32 v2, 0x80000, v2\n"
      "v_subrev_u32 v3, 0x80000, v3\n"
      "s_add_u32 s41, s41, 0x200\n"
      ".Lfk_wq11:\n"
      "buffer_load_dwordx4 v2, s[32:35], s41 offen nt lds\n"
      "s_add_u32 m0, m0, 0x400\n"
      "v_add_u32 v2, 0x20000, v2\n"
      "buffer_load_dwordx4 v3, s[32:35], s41 offen nt lds\n"
      "s_add_u32 m0, m0, 0x400\n"
      "v_add_u32 v3, 0x20000, v3\n"
      "s_cmp_lt_u32 m0, 32768\n"
      "s_cbranch_scc1 .Lfk_wq10\n"
      "v_bfrev_b32 v9, 1\n"
      ".Lfkpq0:\n"
      "s_cbranch_execz .Lfkpp1\n"
      "s_waitcnt lgkmcnt(0)\n"
      "s_mul_i32 s23, s23, 0x9e3779b5\n"
      "s_add_u32 s23, s23, 0x7f4a7c15\n"
      "v_lshrrev_b32 v10, 4, v0\n"
      "v_bfe_u32 v11, v0, 2, 2\n"
      "v_lshl_add_u32 v100, v10, 3, v11\n"
      "v_lshlrev_b32 v100, 7, v100\n"
      "v_lshrrev_b32 v11, 1, v11\n"
      "v_and_b32 v10, 1, v10\n"
      "v_lshl_or_b32 v11, v10, 1, v11\n"
      "v_lshl_or_b32 v100, v11, 5, v100\n"
      "v_and_b32 v10, 3, v0\n"
      "v_lshl_or_b32 v100, v10, 3, v100\n"
      "v_xor_b32 v101, 32, v100\n"
      "v_xor_b32 v102, 64, v100\n"
      "v_xor_b32 v103, 0x60, v100\n"
      "s_waitcnt vmcnt(16)\n"
      "ds_read_b128 v[10:13], v1 offset:10240\n"
      "s_mov_b32 s78, 0x3b124925\n"
      "s_mov_b32 s79, 0x3727c5ac\n"
      "v_and_b32 v22, 48, v0\n"
      "s_lshl_b32 s41, s40, 8\n"
      "v_add_u32 v22, s41, v22\n"
      "s_cmp_eq_u32 s94, 1\n"
      "s_cselect_b32 s41, 0, 0x80\n"
      "v_add_u32 v23, s41, v22\n"
      "s_waitcnt lgkmcnt(0)\n"
      "v_lshlrev_b32 v14, 16, v10\n"
      "v_and_b32 v15, 0xffff0000, v10\n"
      "v_lshlrev_b32 v16, 16, v11\n"
      "v_and_b32 v17, 0xffff0000, v11\n"
      "v_lshlrev_b32 v18, 16, v12\n"
      "v_and_b32 v19, 0xffff0000, v12\n"
      "v_lshlrev_b32 v20, 16, v13\n"
      "v_and_b32 v21, 0xffff0000, v13\n"
      "v_add_f32 v10, v14, v15\n"
      "v_add_f32 v11, v16, v17\n"
      "v_add_f32 v12, v18, v19\n"
      "v_add_f32 v13, v20, v21\n"
      "v_add_f32 v10, v10, v11\n"
      "v_add_f32 v12, v12, v13\n"
      "v_add_f32 v10, v10, v12\n"
      "s_nop 1\n"
      "v_add_f32_dpp v10, v10, v10 row_ror:8 row_mask:0xf bank_mask:0xf\n"
      "s_nop 1\n"
      "v_add_f32_dpp v10, v10, v10 row_ror:4 row_mask:0xf bank_mask:0xf\n"
      "s_nop 1\n"
      "v_add_f32_dpp v10, v10, v10 row_ror:2 row_mask:0xf bank_mask:0xf\n"
      "s_nop 1\n"
      "v_add_f32_dpp v10, v10, v10 row_ror:1 row_mask:0xf bank_mask:0xf\n"
      "v_mov_b32 v11, v10\n"
      "s_nop 1\n"
      "v_permlane16_swap_b32 v10, v11\n"
      "v_add_f32 v10, v10, v11\n"
      "v_mov_b32 v11, v10\n"
      "s_nop 1\n"
      "v_permlane32_swap_b32 v10, v11\n"
      "v_add_f32 v10, v10, v11\n"
      "v_mul_f32 v10, s78, v10\n"
      "s_mov_b32 exec_hi, 0x00ffffff\n"
      "v_sub_f32 v14, v14, v10\n"
      "v_sub_f32 v15, v15, v10\n"
      "v_sub_f32 v16, v16, v10\n"
      "v_sub_f32 v17, v17, v10\n"
      "v_sub_f32 v18, v18, v10\n"
      "v_sub_f32 v19, v19, v10\n"
      "v_sub_f32 v20, v20, v10\n"
      "v_sub_f32 v21, v21, v10\n"
      "s_mov_b32 exec_hi, -1\n"
      "v_mul_f32 v10, v14, v14\n"
      "v_fmac_f32 v10, v15, v15\n"
      "v_fmac_f32 v10, v16, v16\n"
      "v_fmac_f32 v10, v17, v17\n"
      "v_fmac_f32 v10, v18, v18\n"
      "v_fmac_f32 v10, v19, v19\n"
      "v_fmac_f32 v10, v20, v20\n"
      "v_fmac_f32 v10, v21, v21\n"
      "s_nop 1\n"
      "v_add_f32_dpp v10, v10, v10 row_ror:8 row_mask:0xf bank_mask:0xf\n"
      "s_nop 1\n"
      "v_add_f32_dpp v10, v10, v10 row_ror:4 row_mask:0xf bank_mask:0xf\n"
      "s_nop 1\n"
      "v_add_f32_dpp v10, v10, v10 row_ror:2 row_mask:0xf bank_mask:0xf\n"
      "s_nop 1\n"
      "v_add_f32_dpp v10, v10, v10 row_ror:1 row_mask:0xf bank_mask:0xf\n"
      "v_mov_b32 v11, v10\n"
      "s_nop 1\n"
      "v_permlane16_swap_b32 v10, v11\n"
      "v_add_f32 v10, v10, v11\n"
      "v_mov_b32 v11, v10\n"
      "s_nop 1\n"
      "v_permlane32_swap_b32 v10, v11\n"
      "v_add_f32 v10, v10, v11\n"
      "v_mov_b32 v11, s79\n"
      "v_fmac_f32 v11, s78, v10\n"
      "v_rsq_f32 v11, v11\n"
      "s_nop 0\n"
      "v_mul_f32 v14, v14, v11\n"
      "v_mul_f32 v15, v15, v11\n"
      "v_mul_f32 v16, v16, v11\n"
      "v_mul_f32 v17, v17, v11\n"
      "v_mul_f32 v18, v18, v11\n"
      "v_mul_f32 v19, v19, v11\n"
      "v_mul_f32 v20, v20, v11\n"
      "v_mul_f32 v21, v21, v11\n"
      "v_cvt_pk_bf16_f32 v10, v14, v15\n"
      "v_cvt_pk_bf16_f32 v11, v16, v17\n"
      "v_cvt_pk_bf16_f32 v12, v18, v19\n"
      "v_cvt_pk_bf16_f32 v13, v20, v21\n"
      "ds_write_b128 v1, v[10:13] offset:10240\n"
      "ds_read_b128 v[124:127], v22 offset:10240\n"
      "ds_read_b128 v[128:131], v22 offset:10304\n"
      "ds_read_b128 v[132:135], v23 offset:10240\n"
      "ds_read_b128 v[136:139], v23 offset:10304\n"
      "s_mov_b32 s60, 0xffff0000\n"
      "s_mov_b32 s61, 0xffff0000\n"
      "s_mov_b32 s62, 0x0\n"
      "s_mov_b32 s63, 0xffffffff\n"
      "v_and_b32 v2, 1, v0\n"
      "v_cmp_eq_u32 vcc, 0, v2\n"
      "v_lshlrev_b32 v3, 2, v0\n"
      "v_mov_b32 v5, s23\n"
      "v_cndmask_b32 v2, v9, v3, vcc\n"
      "s_lshl_b32 s41, s37, 8\n"
      "s_add_u32 s41, s41, s38\n"
      "s_add_u32 s41, s41, 81920\n"
      ".Lfkpq1:\n"
      "s_cbranch_execz .Lfkpp2\n"
      "s_waitcnt vmcnt(0) lgkmcnt(0)\n"
      "ds_read_b64_tr_b16 v[24:25], v100 offset:16384\n"
      "ds_read_b64_tr_b16 v[26:27], v100 offset:16896\n"
      "ds_read_b64_tr_b16 v[28:29], v101 offset:16384\n"
      "ds_read_b64_tr_b16 v[30:31], v101 offset:16896\n"
      "ds_read_b64_tr_b16 v[32:33], v102 offset:16384\n"
      "ds_read_b64_tr_b16 v[34:35], v102 offset:16896\n"
      "ds_read_b64_tr_b16 v[36:37], v103 offset:16384\n"
      "ds_read_b64_tr_b16 v[38:39], v103 offset:16896\n"
      "ds_read_b64_tr_b16 v[40:41], v100 offset:20480\n"
      "ds_read_b64_tr_b16 v[42:43], v100 offset:20992\n"
      "ds_read_b64_tr_b16 v[44:45], v101 offset:20480\n"
      "ds_read_b64_tr_b16 v[46:47], v101 offset:20992\n"
      "ds_read_b64_tr_b16 v[48:49], v102 offset:20480\n"
      "ds_read_b64_tr_b16 v[50:51], v102 offset:20992\n"
      "ds_read_b64_tr_b16 v[52:53], v103 offset:20480\n"
      "ds_read_b64_tr_b16 v[54:55], v103 offset:20992\n"
      "s_waitcnt lgkmcnt(8)\n"
      "v_mfma_f32_16x16x32_bf16 v[104:107], v[124:127], v[24:27], 0\n"
      "v_mfma_f32_16x16x32_bf16 v[108:111], v[124:127], v[28:31], 0\n"
      "v_mfma_f32_16x16x32_bf16 v[112:115], v[124:127], v[32:35], 0\n"
      "v_mfma_f32_16x16x32_bf16 v[116:119], v[124:127], v[36:39], 0\n"
      "ds_read_b64_tr_b16 v[56:57], v100 offset:24576\n"
      "ds_read_b64_tr_b16 v[58:59], v100 offset:25088\n"
      "ds_read_b64_tr_b16 v[60:61], v101 offset:24576\n"
      "ds_read_b64_tr_b16 v[62:63], v101 offset:25088\n"
      "ds_read_b64_tr_b16 v[64:65], v102 offset:24576\n"
      "ds_read_b64_tr_b16 v[66:67], v102 offset:25088\n"
      "ds_read_b64_tr_b16 v[68:69], v103 offset:24576\n"
      "ds_read_b64_tr_b16 v[70:71], v103 offset:25088\n"
      "s_waitcnt lgkmcnt(8)\n"
      "v_mfma_f32_16x16x32_bf16 v[104:107], v[128:131], v[40:43], v[104:107]\n"
      "v_mfma_f32_16x16x32_bf16 v[108:111], v[128:131], v[44:47], v[108:111]\n"
      "v_mfma_f32_16x16x32_bf16 v[112:115], v[128:131], v[48:51], v[112:115]\n"
      "v_mfma_f32_16x16x32_bf16 v[116:119], v[128:131], v[52:55], v[116:119]\n"
      "ds_read_b64_tr_b16 v[72:73], v100 offset:28672\n"
      "ds_read_b64_tr_b16 v[74:75], v100 offset:29184\n"
      "ds_read_b64_tr_b16 v[76:77], v101 offset:28672\n"
      "ds_read_b64_tr_b16 v[78:79], v101 offset:29184\n"
      "ds_read_b64_tr_b16 v[80:81], v102 offset:28672\n"
      "ds_read_b64_tr_b16 v[82:83], v102 offset:29184\n"
      "ds_read_b64_tr_b16 v[84:85], v103 offset:28672\n"
      "ds_read_b64_tr_b16 v[86:87], v103 offset:29184\n"
      "s_waitcnt lgkmcnt(8)\n"
      "v_mfma_f32_16x16x32_bf16 v[140:143], v[132:135], v[56:59], 0\n"
      "v_mfma_f32_16x16x32_bf16 v[144:147], v[132:135], v[60:63], 0\n"
      "v_mfma_f32_16x16x32_bf16 v[148:151], v[132:135], v[64:67], 0\n"
      "v_mfma_f32_16x16x32_bf16 v[152:155], v[132:135], v[68:71], 0\n"
      "s_waitcnt lgkmcnt(0)\n"
      "v_mfma_f32_16x16x32_bf16 v[140:143], v[136:139], v[72:75], v[140:143]\n"
      "v_mfma_f32_16x16x32_bf16 v[144:147], v[136:139], v[76:79], v[144:147]\n"
      "v_mfma_f32_16x16x32_bf16 v[148:151], v[136:139], v[80:83], v[148:151]\n"
      "v_mfma_f32_16x16x32_bf16 v[152:155], v[136:139], v[84:87], v[152:155]\n"
      "v_cndmask_b32 v4, v104, v108, s[60:61]\n"
      "v_cndmask_b32 v6, v112, v116, s[60:61]\n"
      "v_cndmask_b32 v4, v4, v6, s[62:63]\n"
      "s_nop 4\n"
      "v_cndmask_b32 v6, v140, v144, s[60:61]\n"
      "v_cndmask_b32 v8, v148, v152, s[60:61]\n"
      "v_cndmask_b32 v6, v6, v8, s[62:63]\n"
      "s_cmp_eq_u32 s94, 1\n"
      "s_cbranch_scc1 .Lfk_du13\n"
      "v_add_f32 v4, v4, v6\n"
      "s_branch .Lfk_du14\n"
      ".Lfk_du13:\n"
      "v_mov_b32 v7, s23\n"
      "s_add_u32 s43, s41, 0x1000\n"
      "s_nop 1\n"
      "v_mov_b32_dpp v156, v6 quad_perm:[1,0,3,2] row_mask:0xf bank_mask:0xf\n"
      "v_cvt_pk_bf16_f32 v6, v6, v156\n"
      "buffer_store_dwordx2 v[6:7], v2, s[24:27], s43 offen sc1\n"
      ".Lfk_du14:\n"
      "s_nop 1\n"
      "v_mov_b32_dpp v156, v4 quad_perm:[1,0,3,2] row_mask:0xf bank_mask:0xf\n"
      "v_cvt_pk_bf16_f32 v4, v4, v156\n"
      "buffer_store_dwordx2 v[4:5], v2, s[24:27], s41 offen sc1\n"
      "s_setprio 0\n"
      "v_mov_b32 v120, 0x3f803f80\n"
      "v_mov_b32 v121, 0x3f803f80\n"
      "v_mov_b32 v122, 0x3f803f80\n"
      "v_mov_b32 v123, 0x3f803f80\n"
      "s_nop 1\n"
      "v_mfma_f32_16x16x32_bf16 v[104:107], v[120:123], v[24:27], 0\n"
      "v_mfma_f32_16x16x32_bf16 v[108:111], v[120:123], v[28:31], 0\n"
      "v_mfma_f32_16x16x32_bf16 v[112:115], v[120:123], v[32:35], 0\n"
      "v_mfma_f32_16x16x32_bf16 v[116:119], v[120:123], v[36:39], 0\n"
      "v_mfma_f32_16x16x32_bf16 v[104:107], v[120:123], v[40:43], v[104:107]\n"
      "v_mfma_f32_16x16x32_bf16 v[108:111], v[120:123], v[44:47], v[108:111]\n"
      "v_mfma_f32_16x16x32_bf16 v[112:115], v[120:123], v[48:51], v[112:115]\n"
      "v_mfma_f32_16x16x32_bf16 v[116:119], v[120:123], v[52:55], v[116:119]\n"
      "v_mfma_f32_16x16x32_bf16 v[140:143], v[120:123], v[56:59], 0\n"
      "v_mfma_f32_16x16x32_bf16 v[144:147], v[120:123], v[60:63], 0\n"
      "v_mfma_f32_16x16x32_bf16 v[148:151], v[120:123], v[64:67], 0\n"
      "v_mfma_f32_16x16x32_bf16 v[152:155], v[120:123], v[68:71], 0\n"
      "v_mfma_f32_16x16x32_bf16 v[140:143], v[120:123], v[72:75], v[140:143]\n"
      "v_mfma_f32_16x16x32_bf16 v[144:147], v[120:123], v[76:79], v[144:147]\n"
      "v_mfma_f32_16x16x32_bf16 v[148:151], v[120:123], v[80:83], v[148:151]\n"
      "v_mfma_f32_16x16x32_bf16 v[152:155], v[120:123], v[84:87], v[152:155]\n"
      "v_cndmask_b32 v6, v104, v108, s[60:61]\n"
      "v_cndmask_b32 v7, v112, v116, s[60:61]\n"
      "v_cndmask_b32 v6, v6, v7, s[62:63]\n"
      "s_nop 4\n"
      "v_cndmask_b32 v2, v140, v144, s[60:61]\n"
      "v_cndmask_b32 v7, v148, v152, s[60:61]\n"
      "v_cndmask_b32 v2, v2, v7, s[62:63]\n"
      "v_mov_b32 v119, v6\n"
      "s_branch .Lfk_proj15\n"
      ".Lfk_qn9:\n"
      "s_setprio 0\n"
      "s_sub_u32 s40, s37, 19\n"
      "s_lshl_b32 s40, s40, 1\n"
      "s_add_u32 s40, s40, s36\n"
      "s_cmp_lt_u32 s40, 28\n"
      "s_cbranch_scc0 .Lfk_proj2\n"
      "v_mov_b32 v120, -1\n"
      "v_mov_b32 v121, -1\n"
      "v_mov_b32 v122, -1\n"
      "v_mov_b32 v123, -1\n"
      "v_lshrrev_b32 v124, 3, v0\n"
      "v_and_b32 v125, 7, v0\n"
      "v_lshlrev_b32 v125, 4, v125\n"
      "v_lshl_or_b32 v124, v124, 12, v125\n"
      "s_lshl_b32 s42, s40, 7\n"
      "s_add_u32 s42, s42, 94208\n"
      "s_add_u32 s41, s42, 28672\n"
      "s_mov_b32 exec_hi, 0xffffff\n"
      "buffer_store_dwordx4 v[120:123], v124, s[24:27], s42 offen sc1\n"
      "buffer_store_dwordx4 v[120:123], v124, s[24:27], s41 offen sc1\n"
      "s_cmp_eq_u32 s40, 0\n"
      "s_cbranch_scc0 .Lfk_cc16\n"
      "buffer_store_dwordx4 v[120:123], v124, s[24:27], s42 offen offset:3584 sc1\n"
      "buffer_store_dwordx4 v[120:123], v124, s[24:27], s41 offen offset:3584 sc1\n"
      ".Lfk_cc16:\n"
      "s_mov_b32 exec_hi, -1\n"
      "s_waitcnt vmcnt(0)\n"
      "s_branch .Lfk_proj2\n"
      "s_branch .Lfkpp0\n"
      ".p2align 12\n"
      ".Lfkpp0:\n"
      "s_cbranch_execz .Lfkpf0a\n"
      ".Lfk_proj2:\n"
      "s_waitcnt lgkmcnt(0)\n"
      "s_mul_i32 s23, s23, 0x9e3779b5\n"
      "s_add_u32 s23, s23, 0x7f4a7c15\n"
      ".Lfk_proj15:\n"
      "v_lshrrev_b32 v10, 4, v0\n"
      "v_bfe_u32 v11, v0, 2, 2\n"
      "v_lshl_add_u32 v100, v10, 3, v11\n"
      "v_lshlrev_b32 v100, 7, v100\n"
      "v_lshrrev_b32 v11, 1, v11\n"
      "v_and_b32 v10, 1, v10\n"
      "v_lshl_or_b32 v11, v10, 1, v11\n"
      "v_lshl_or_b32 v100, v11, 5, v100\n"
      "v_and_b32 v10, 3, v0\n"
      "v_lshl_or_b32 v100, v10, 3, v100\n"
      "v_xor_b32 v101, 32, v100\n"
      "v_xor_b32 v102, 64, v100\n"
      "v_xor_b32 v103, 0x60, v100\n"
      "s_mov_b32 s60, 0xffff0000\n"
      "s_mov_b32 s61, 0xffff0000\n"
      "s_mov_b32 s62, 0x0\n"
      "s_mov_b32 s63, 0xffffffff\n"
      "s_mov_b32 s64, 0xff00ff00\n"
      "s_mov_b32 s65, 0xff00ff00\n"
      "s_sub_u32 s39, s22, 4\n"
      "s_mov_b32 s76, 0x3f80\n"
      "s_mov_b32 s77, 0x3f800000\n"
      "s_mov_b32 s78, 0x3b124925\n"
      "s_mov_b32 s79, 0x3727c5ac\n"
      "s_mov_b32 s80, 0xbfb8aa3b\n"
      "s_lshl_b32 s40, s39, 7\n"
      "v_and_b32 v10, 7, v0\n"
      "v_lshrrev_b32 v11, 3, v0\n"
      "v_lshlrev_b32 v10, 4, v10\n"
      "v_lshl_or_b32 v10, v11, 1, v10\n"
      "v_add_u32 v104, s40, v10\n"
      "v_and_b32 v11, 48, v0\n"
      "v_add_u32 v105, s40, v11\n"
      "v_lshl_add_u32 v106, v0, 1, s40\n"
      "v_lshlrev_b32 v109, 2, v0\n"
      "s_lshl_b32 s41, s39, 8\n"
      "v_add_u32 v108, s41, v109\n"
      "v_bfrev_b32 v9, 1\n"
      "v_and_b32 v10, 1, v0\n"
      "v_cmp_eq_u32 vcc, 0, v10\n"
      "v_lshlrev_b32 v11, 1, v0\n"
      "s_sub_u32 s82, s37, 19\n"
      "v_cndmask_b32 v110, v9, v11, vcc\n"
      "v_cndmask_b32 v111, v9, v109, vcc\n"
      "v_cmp_gt_u32 vcc, 32, v0\n"
      "s_and_b32 s41, s82, 3\n"
      "s_cselect_b32 s82, 64, s82\n"
      "s_lshl_b32 s82, s82, 1\n"
      "s_add_u32 s82, s82, s36\n"
      "v_cndmask_b32 v112, v9, v109, vcc\n"
      "v_cmp_gt_u32 vcc, 40, v0\n"
      "s_sub_u32 s41, s37, 16\n"
      "s_max_i32 s41, s41, 0\n"
      "v_cndmask_b32 v113, v9, v1, vcc\n"
      "s_lshr_b32 s41, s41, 2\n"
      "s_sub_u32 s41, s37, s41\n"
      "s_and_b32 s83, s41, 3\n"
      "s_lshr_b32 s84, s41, 2\n"
      "s_lshl_b32 s85, s36, 2\n"
      "s_add_u32 s85, s85, s83\n"
      "s_cmp_gt_u32 s82, 31\n"
      "s_cselect_b32 s86, 1, 0\n"
      "s_cmp_lt_u32 s82, 28\n"
      "s_cselect_b32 s86, 2, s86\n"
      "s_lshr_b32 s87, s39, 1\n"
      "s_and_b32 s88, s39, 1\n"
      "s_and_b32 s89, s37, 3\n"
      "v_lshrrev_b32 v11, 3, v0\n"
      "v_and_b32 v12, 2, v11\n"
      "v_and_b32 v10, 7, v0\n"
      "v_xor_b32 v12, v12, v10\n"
      "v_lshlrev_b32 v12, 4, v12\n"
      "v_xor_b32 v13, 64, v12\n"
      ".Lfkpp1:\n"
      "s_cbranch_execz .Lfkpf0f\n"
      "s_waitcnt lgkmcnt(0)\n"
      "s_barrier\n"
      "s_cmp_eq_u32 s39, 6\n"
      "s_cbranch_scc0 .Lfk_cl20\n"
      "v_mov_b32 v120, -1\n"
      "v_mov_b32 v121, -1\n"
      "v_mov_b32 v122, -1\n"
      "v_mov_b32 v123, -1\n"
      "v_lshrrev_b32 v124, 3, v0\n"
      "v_and_b32 v125, 7, v0\n"
      "v_lshlrev_b32 v125, 4, v125\n"
      "v_mul_u32_u24 v126, 28672, v124\n"
      "v_add_u32 v126, v126, v125\n"
      "v_lshl_or_b32 v124, v124, 12, v125\n"
      "s_lshl_b32 s40, s37, 7\n"
      "s_add_u32 s40, s40, s38\n"
      "buffer_store_dwordx4 v[120:123], v124, s[24:27], s40 offen sc1\n"
      "s_cmp_eq_u32 s86, 1\n"
      "s_cbranch_scc0 .Lfk_cl21\n"
      "s_lshl_b32 s40, s84, 12\n"
      "s_lshl_b32 s41, s85, 7\n"
      "s_add_u32 s40, s40, s41\n"
      "s_add_u32 s40, s40, 36864\n"
      "s_mov_b64 exec, 0xffff\n"
      "buffer_store_dwordx4 v[120:123], v126, s[24:27], s40 offen sc1\n"
      ".Lfk_cl21:\n"
      "s_mov_b64 exec, -1\n"
      ".Lfk_cl20:\n"
      "s_cmp_eq_u32 s86, 1\n"
      "s_cbranch_scc1 .Lfk_o17\n"
      "s_cmp_eq_u32 s86, 2\n"
      "s_cbranch_scc0 .Lfk_w19\n"
      "s_cmp_eq_u32 s39, 7\n"
      "s_cbranch_scc1 .Lfk_w19\n"
      "s_mov_b32 s28, s14\n"
      "s_and_b32 s29, s15, 0xffff\n"
      "s_mov_b32 s30, 1605632\n"
      "s_mov_b32 s31, 0x20000\n"
      "s_mul_i32 s40, s39, 229376\n"
      "s_lshl_b32 s41, s82, 7\n"
      "s_add_u32 s40, s40, s41\n"
      "s_mul_i32 s41, s39, 8192\n"
      "s_add_u32 m0, s41, 32768\n"
      "v_mul_u32_u24 v10, 3584, v11\n"
      "v_add_u32 v14, v10, v12\n"
      "v_add_u32 v15, v10, v13\n"
      "buffer_load_dwordx4 v14, s[28:31], s40 offen nt lds\n"
      "s_add_u32 m0, m0, 0x400\n"
      "s_add_u32 s40, s40, 28672\n"
      "buffer_load_dwordx4 v15, s[28:31], s40 offen nt lds\n"
      "s_add_u32 m0, m0, 0x400\n"
      "s_add_u32 s40, s40, 28672\n"
      "buffer_load_dwordx4 v14, s[28:31], s40 offen nt lds\n"
      "s_add_u32 m0, m0, 0x400\n"
      "s_add_u32 s40, s40, 28672\n"
      "buffer_load_dwordx4 v15, s[28:31], s40 offen nt lds\n"
      "s_branch .Lfk_w19\n"
      ".Lfk_o17:\n"
      "s_mov_b32 s28, s12\n"
      "s_and_b32 s29, s13, 0xffff\n"
      "s_mov_b32 s30, 3670016\n"
      "s_mov_b32 s31, 0x20000\n"
      "s_lshl_b32 s40, s85, 7\n"
      "s_lshl_b32 s41, s39, 4\n"
      "s_add_u32 s40, s40, s41\n"
      "s_mul_i32 s40, s40, 896\n"
      "s_lshl_b32 s41, s84, 7\n"
      "s_add_u32 s40, s40, s41\n"
      "s_mul_i32 s41, s39, 2048\n"
      "s_add_u32 m0, s41, 32768\n"
      "v_mul_u32_u24 v10, 896, v11\n"
      "v_add_u32 v14, v10, v12\n"
      "v_add_u32 v15, v10, v13\n"
      "buffer_load_dwordx4 v14, s[28:31], s40 offen nt lds\n"
      "s_add_u32 m0, m0, 0x400\n"
      "s_add_u32 s40, s40, 7168\n"
      "buffer_load_dwordx4 v15, s[28:31], s40 offen nt lds\n"
      ".Lfk_w19:\n"
      "s_cmp_eq_u32 s94, 2\n"
      "s_cselect_b32 s89, 9, s89\n"
      "s_cmp_eq_u32 s87, s89\n"
      "s_cbranch_scc0 .Lfk_nq23\n"
      "s_lshl_b32 s40, s88, 13\n"
      "v_add_u32 v120, s40, v100\n"
      "v_add_u32 v121, s40, v101\n"
      "v_add_u32 v122, s40, v102\n"
      "v_add_u32 v123, s40, v103\n"
      "ds_read_b64_tr_b16 v[68:69], v120 offset:16384\n"
      "ds_read_b64_tr_b16 v[70:71], v120 offset:16896\n"
      "ds_read_b64_tr_b16 v[72:73], v121 offset:16384\n"
      "ds_read_b64_tr_b16 v[74:75], v121 offset:16896\n"
      "ds_read_b64_tr_b16 v[76:77], v122 offset:16384\n"
      "ds_read_b64_tr_b16 v[78:79], v122 offset:16896\n"
      "ds_read_b64_tr_b16 v[80:81], v123 offset:16384\n"
      "ds_read_b64_tr_b16 v[82:83], v123 offset:16896\n"
      "ds_read_b64_tr_b16 v[84:85], v120 offset:20480\n"
      "ds_read_b64_tr_b16 v[86:87], v120 offset:20992\n"
      "ds_read_b64_tr_b16 v[88:89], v121 offset:20480\n"
      "ds_read_b64_tr_b16 v[90:91], v121 offset:20992\n"
      "ds_read_b64_tr_b16 v[92:93], v122 offset:20480\n"
      "ds_read_b64_tr_b16 v[94:95], v122 offset:20992\n"
      "ds_read_b64_tr_b16 v[96:97], v123 offset:20480\n"
      "ds_read_b64_tr_b16 v[98:99], v123 offset:20992\n"
      ".Lfk_nq23:\n"
      ".Lfkpp2:\n"
      "s_cbranch_execz .Lfkpf0b\n"
      "s_cmp_eq_u32 s39, 6\n"
      "s_cbranch_scc0 .Lfk_cw24\n"
      "s_waitcnt vmcnt(4)\n"
      "s_cmp_eq_u32 s86, 2\n"
      "s_cbranch_scc1 .Lfk_cw24\n"
      "s_waitcnt vmcnt(2)\n"
      "s_cmp_eq_u32 s86, 1\n"
      "s_cbranch_scc1 .Lfk_cw24\n"
      "s_waitcnt vmcnt(0)\n"
      ".Lfk_cw24:\n"
      "s_lshl_b32 s50, s36, 12\n"
      "s_add_u32 s50, s50, 1077248\n"
      "s_lshl_b32 s40, s37, 7\n"
      "s_add_u32 s51, s50, s40\n"
      "v_mov_b32 v120, s23\n"
      "v_mov_b32 v122, s23\n"
      "s_waitcnt lgkmcnt(0)\n"
      "s_barrier\n"
      "s_cmp_eq_u32 s39, 6\n"
      "s_cbranch_scc0 .Lfk_n730\n"
      "s_memrealtime s[92:93]\n"
      "s_waitcnt lgkmcnt(0)\n"
      "v_mov_b32 v121, s92\n"
      "v_mov_b32 v123, s92\n"
      "s_mov_b64 exec, 0xff\n"
      "buffer_store_dwordx4 v[120:123], v1, s[24:27], s51 offen sc1\n"
      "s_mov_b64 exec, -1\n"
      "s_cmp_eq_u32 s86, 2\n"
      "s_cbranch_scc1 .Lfk_F26\n"
      "s_waitcnt vmcnt(1)\n"
      "s_branch .Lfk_n731\n"
      ".Lfk_n730:\n"
      "s_cmp_eq_u32 s86, 2\n"
      "s_cbranch_scc1 .Lfk_F26\n"
      "s_cmp_eq_u32 s39, 7\n"
      "s_cbranch_scc0 .Lfk_n728\n"
      "s_cmp_eq_u32 s86, 0\n"
      "s_cbranch_scc1 .Lfk_n731\n"
      "v_lshlrev_b32 v124, 7, v0\n"
      "s_mov_b32 s59, 65536\n"
      "s_mov_b32 exec_lo, 0xffffffff\n"
      "s_mov_b32 exec_hi, 0\n"
      ".Lfk_ls32:\n"
      "buffer_load_dwordx3 v[120:122], v124, s[24:27], s50 offen sc1\n"
      "s_waitcnt vmcnt(0)\n"
      "v_xor_b32 v120, s23, v120\n"
      "v_xor_b32 v122, s23, v122\n"
      "v_or_b32 v120, v120, v122\n"
      "v_cmp_ne_u32 vcc, 0, v120\n"
      "s_and_b64 exec, vcc, exec\n"
      "s_cbranch_scc0 .Lfk_ld33\n"
      "s_sub_u32 s59, s59, 1\n"
      "s_cbranch_scc0 .Lfk_ls32\n"
      ".Lfk_ld33:\n"
      "s_mov_b64 exec, -1\n"
      "s_cmp_eq_u32 s37, 0\n"
      "s_cbranch_scc0 .Lfk_n731\n"
      "s_memrealtime s[92:93]\n"
      "v_lshrrev_b32 v124, 3, v0\n"
      "v_and_b32 v125, 7, v0\n"
      "v_lshlrev_b32 v125, 4, v125\n"
      "v_lshl_or_b32 v124, v124, 10, v125\n"
      "s_lshl_b32 s40, s36, 7\n"
      "s_add_u32 s40, s40, 1110016\n"
      "v_readlane_b32 s41, v121, 19\n"
      "v_readlane_b32 s42, v121, 23\n"
      "v_readlane_b32 s43, v121, 27\n"
      "v_readlane_b32 s44, v121, 31\n"
      "v_mov_b32 v120, s23\n"
      "v_mov_b32 v122, s23\n"
      "s_cmp_lt_u32 s36, 4\n"
      "s_cselect_b32 s44, s44, s43\n"
      "s_waitcnt lgkmcnt(0)\n"
      "s_sub_u32 s41, s41, s92\n"
      "s_sub_u32 s42, s42, s92\n"
      "s_sub_u32 s43, s43, s92\n"
      "s_sub_u32 s44, s44, s92\n"
      "s_max_i32 s41, s41, s42\n"
      "s_max_i32 s43, s43, s44\n"
      "s_max_i32 s41, s41, s43\n"
      "s_add_u32 s41, s41, 0\n"
      "s_max_i32 s41, s41, 0\n"
      "s_add_u32 s92, s92, s41\n"
      "v_mov_b32 v121, s92\n"
      "v_mov_b32 v123, s92\n"
      "buffer_store_dwordx4 v[120:123], v124, s[24:27], s40 offen sc1\n"
      "s_branch .Lfk_n731\n"
      ".Lfk_n728:\n"
      ".Lfkpp3:\n"
      "s_cbranch_execz .Lfkpf0c\n"
      "s_lshl_b32 s50, s83, 13\n"
      "s_lshl_b32 s40, s39, 11\n"
      "s_add_u32 s50, s50, s40\n"
      "s_add_u32 s50, s50, s38\n"
      "s_add_u32 s50, s50, 0\n"
      "s_add_u32 s51, s40, 98304\n"
      "s_lshr_b32 s52, s83, 1\n"
      "s_lshl_b32 s52, s52, 12\n"
      "s_add_u32 s52, s52, s38\n"
      "s_add_u32 s52, s52, 65536\n"
      "s_lshl_b32 s53, s84, 12\n"
      "s_lshl_b32 s40, s85, 7\n"
      "s_add_u32 s53, s53, s40\n"
      "s_add_u32 s53, s53, 36864\n"
      "s_mov_b32 s59, 65536\n"
      "v_lshrrev_b32 v10, 4, v0\n"
      "v_and_b32 v11, 3, v0\n"
      "v_lshl_add_u32 v11, v10, 2, v11\n"
      "s_lshl_b32 s40, s88, 4\n"
      "v_add_u32 v11, s40, v11\n"
      "v_lshlrev_b32 v114, 7, v11\n"
      "v_and_b32 v11, 15, v0\n"
      "v_lshrrev_b32 v12, 1, v11\n"
      "v_xor_b32 v10, v10, v12\n"
      "v_lshlrev_b32 v10, 4, v10\n"
      "v_lshl_or_b32 v10, v11, 7, v10\n"
      "v_add_u32 v115, s51, v10\n"
      "v_xor_b32 v116, 64, v115\n"
      "s_lshl_b32 s40, s87, 13\n"
      "v_add_u32 v142, s40, v100\n"
      "v_add_u32 v143, s40, v101\n"
      "v_add_u32 v144, s40, v102\n"
      "v_add_u32 v145, s40, v103\n"
      "s_waitcnt vmcnt(0)\n"
      ".Lfk_n731:\n"
      "s_waitcnt lgkmcnt(0)\n"
      "s_barrier\n"
      "s_cmp_eq_u32 s86, 1\n"
      "s_cbranch_scc0 .Lfk_X27\n"
      ".Lfkpo0a:\n"
      "s_cbranch_execz .Lfkpf0e\n"
      "s_cmp_gt_u32 s39, 3\n"
      "s_cbranch_scc1 .Lfk_bo34\n"
      ".Lfk_og35:\n"
      "s_mov_b32 m0, s51\n"
      "s_nop 0\n"
      "buffer_load_dwordx4 v1, s[24:27], s50 offen sc1 lds\n"
      "buffer_load_dwordx4 v1, s[24:27], s50 offen offset:1024 sc1 lds\n"
      "buffer_load_dwordx4 v[124:127], v114, s[24:27], s52 offen sc1\n"
      "ds_read_b64_tr_b16 v[4:5], v142 offset:32768\n"
      "ds_read_b64_tr_b16 v[6:7], v142 offset:33280\n"
      "ds_read_b64_tr_b16 v[8:9], v143 offset:32768\n"
      "ds_read_b64_tr_b16 v[10:11], v143 offset:33280\n"
      "ds_read_b64_tr_b16 v[12:13], v144 offset:32768\n"
      "ds_read_b64_tr_b16 v[14:15], v144 offset:33280\n"
      "ds_read_b64_tr_b16 v[16:17], v145 offset:32768\n"
      "ds_read_b64_tr_b16 v[18:19], v145 offset:33280\n"
      "ds_read_b64_tr_b16 v[20:21], v142 offset:36864\n"
      "ds_read_b64_tr_b16 v[22:23], v142 offset:37376\n"
      "ds_read_b64_tr_b16 v[24:25], v143 offset:36864\n"
      "ds_read_b64_tr_b16 v[26:27], v143 offset:37376\n"
      "ds_read_b64_tr_b16 v[28:29], v144 offset:36864\n"
      "ds_read_b64_tr_b16 v[30:31], v144 offset:37376\n"
      "ds_read_b64_tr_b16 v[32:33], v145 offset:36864\n"
      "ds_read_b64_tr_b16 v[34:35], v145 offset:37376\n"
      "s_waitcnt vmcnt(0)\n"
      "ds_read_b128 v[128:131], v115\n"
      "ds_read_b128 v[132:135], v116\n"
      "v_max_f32_dpp v136, v124, v124 quad_perm:[1,0,3,2] row_mask:0xf bank_mask:0xf\n"
      "v_xor_b32 v125, s23, v125\n"
      "v_xor_b32 v127, s23, v127\n"
      "v_max_f32_dpp v136, v136, v136 quad_perm:[2,3,0,1] row_mask:0xf bank_mask:0xf\n"
      "v_or_b32 v125, v125, v127\n"
      "v_mov_b32 v137, v136\n"
      "v_cmp_ne_u32 s[56:57], 0, v125\n"
      "s_nop 0\n"
      "v_permlane16_swap_b32 v136, v137\n"
      "v_max_f32 v136, v136, v137\n"
      "v_mov_b32 v137, v136\n"
      "s_waitcnt lgkmcnt(1)\n"
      "v_mfma_f32_16x16x32_bf16 v[148:151], v[128:131], v[4:7], 0\n"
      "v_max3_u32 v138, v128, v129, v130\n"
      "v_mfma_f32_16x16x32_bf16 v[152:155], v[128:131], v[8:11], 0\n"
      "v_permlane32_swap_b32 v136, v137\n"
      "v_max_f32 v136, v136, v137\n"
      "v_sub_f32 v137, v124, v136\n"
      "v_mfma_f32_16x16x32_bf16 v[156:159], v[128:131], v[12:15], 0\n"
      "v_exp_f32 v137, v137\n"
      "v_mfma_f32_16x16x32_bf16 v[160:163], v[128:131], v[16:19], 0\n"
      "v_max_u32 v138, v138, v131\n"
      "s_waitcnt lgkmcnt(0)\n"
      "v_mfma_f32_16x16x32_bf16 v[148:151], v[132:135], v[20:23], v[148:151]\n"
      "v_add_f32_dpp v139, v137, v137 quad_perm:[1,0,3,2] row_mask:0xf bank_mask:0xf\n"
      "v_max3_u32 v138, v138, v132, v133\n"
      "v_mfma_f32_16x16x32_bf16 v[152:155], v[132:135], v[24:27], v[152:155]\n"
      "v_add_f32_dpp v139, v139, v139 quad_perm:[2,3,0,1] row_mask:0xf bank_mask:0xf\n"
      "v_max3_u32 v138, v138, v134, v135\n"
      "v_mfma_f32_16x16x32_bf16 v[156:159], v[132:135], v[28:31], v[156:159]\n"
      "v_mov_b32 v140, v139\n"
      "v_cmp_eq_u32 vcc, -1, v138\n"
      "v_mfma_f32_16x16x32_bf16 v[160:163], v[132:135], v[32:35], v[160:163]\n"
      "v_permlane16_swap_b32 v139, v140\n"
      "s_or_b64 vcc, vcc, s[56:57]\n"
      "s_cbranch_vccnz .Lfk_ort37\n"
      ".Lfk_od36:\n"
      "v_add_f32 v139, v139, v140\n"
      "s_nop 1\n"
      "v_mov_b32 v140, v139\n"
      "v_mul_f32_dpp v128, v137, v148 quad_perm:[0,0,0,0] row_mask:0xf bank_mask:0xf\n"
      "v_mul_f32_dpp v130, v137, v156 quad_perm:[0,0,0,0] row_mask:0xf bank_mask:0xf\n"
      "v_mul_f32_dpp v129, v137, v152 quad_perm:[0,0,0,0] row_mask:0xf bank_mask:0xf\n"
      "v_mul_f32_dpp v131, v137, v160 quad_perm:[0,0,0,0] row_mask:0xf bank_mask:0xf\n"
      "v_permlane32_swap_b32 v139, v140\n"
      "v_add_f32 v139, v139, v140\n"
      "v_fmac_f32_dpp v128, v137, v149 quad_perm:[1,1,1,1] row_mask:0xf bank_mask:0xf\n"
      "v_fmac_f32_dpp v130, v137, v157 quad_perm:[1,1,1,1] row_mask:0xf bank_mask:0xf\n"
      "v_fmac_f32_dpp v129, v137, v153 quad_perm:[1,1,1,1] row_mask:0xf bank_mask:0xf\n"
      "v_fmac_f32_dpp v131, v137, v161 quad_perm:[1,1,1,1] row_mask:0xf bank_mask:0xf\n"
      "v_fmac_f32_dpp v128, v137, v150 quad_perm:[2,2,2,2] row_mask:0xf bank_mask:0xf\n"
      "v_fmac_f32_dpp v130, v137, v158 quad_perm:[2,2,2,2] row_mask:0xf bank_mask:0xf\n"
      "v_fmac_f32_dpp v129, v137, v154 quad_perm:[2,2,2,2] row_mask:0xf bank_mask:0xf\n"
      "v_fmac_f32_dpp v131, v137, v162 quad_perm:[2,2,2,2] row_mask:0xf bank_mask:0xf\n"
      "v_fmac_f32_dpp v128, v137, v151 quad_perm:[3,3,3,3] row_mask:0xf bank_mask:0xf\n"
      "v_fmac_f32_dpp v130, v137, v159 quad_perm:[3,3,3,3] row_mask:0xf bank_mask:0xf\n"
      "v_fmac_f32_dpp v129, v137, v155 quad_perm:[3,3,3,3] row_mask:0xf bank_mask:0xf\n"
      "v_fmac_f32_dpp v131, v137, v163 quad_perm:[3,3,3,3] row_mask:0xf bank_mask:0xf\n"
      "s_lshl_b32 s40, s88, 2\n"
      "s_add_u32 s40, s40, 1344\n"
      "v_mov_b32 v140, s40\n"
      "v_permlane32_swap_b32 v128, v130\n"
      "v_permlane32_swap_b32 v129, v131\n"
      "v_add_f32 v128, v128, v130\n"
      "v_add_f32 v129, v129, v131\n"
      "s_mov_b64 exec, 1\n"
      "ds_write2_b32 v140, v136, v139 offset0:0 offset1:2\n"
      "s_mov_b64 exec, -1\n"
      "v_permlane16_swap_b32 v128, v129\n"
      "v_add_f32 v128, v128, v129\n"
      "ds_write_b32 v108, v128 offset:12288\n"
      ".Lfk_bo34:\n"
      ".Lfkpo0c:\n"
      "s_cbranch_execz .Lfkpx0\n"
      "s_waitcnt lgkmcnt(0)\n"
      "s_barrier\n"
      "s_cmp_eq_u32 s39, 0\n"
      "s_cbranch_scc0 .Lfk_oe38\n"
      "v_mov_b32 v120, 0\n"
      "ds_read_b128 v[124:127], v120 offset:1344\n"
      "ds_read2st64_b32 v[128:129], v109 offset0:48 offset1:49\n"
      "ds_read2st64_b32 v[130:131], v109 offset0:50 offset1:51\n"
      "s_waitcnt lgkmcnt(2)\n"
      "v_max_f32 v136, v124, v125\n"
      "v_sub_f32 v124, v124, v136\n"
      "v_sub_f32 v125, v125, v136\n"
      "v_exp_f32 v124, v124\n"
      "v_exp_f32 v125, v125\n"
      "s_waitcnt lgkmcnt(0)\n"
      "v_pk_add_f32 v[128:129], v[128:129], v[130:131]\n"
      "v_pk_mul_f32 v[126:127], v[126:127], v[124:125]\n"
      "v_pk_mul_f32 v[128:129], v[128:129], v[124:125]\n"
      "v_add_f32 v126, v126, v127\n"
      "v_rcp_f32 v126, v126\n"
      "v_add_f32 v128, v128, v129\n"
      "v_mul_f32 v128, v128, v126\n"
      "s_nop 1\n"
      "v_mov_b32_dpp v129, v128 quad_perm:[1,0,3,2] row_mask:0xf bank_mask:0xf\n"
      "v_cvt_pk_bf16_f32 v128, v128, v129\n"
      "v_min_u32 v128, -2, v128\n"
      "buffer_store_dword v128, v110, s[24:27], s53 offen sc1\n"
      "v_lshrrev_b32 v124, 5, v0\n"
      "v_and_b32 v125, 15, v0\n"
      "v_lshlrev_b32 v124, 8, v124\n"
      "v_lshl_or_b32 v124, v125, 4, v124\n"
      "v_bfe_u32 v125, v0, 4, 1\n"
      "v_lshl_or_b32 v124, v125, 3, v124\n"
      "s_lshl_b32 s40, s37, 9\n"
      "s_add_u32 s40, s40, s38\n"
      "s_add_u32 s40, s40, 98304\n"
      "v_mov_b32 v127, s23\n"
      "v_add_f32 v126, v119, v2\n"
      "s_cmp_eq_u32 s94, 1\n"
      "s_cbranch_scc0 .Lfk_cs39\n"
      "v_mov_b32 v128, v2\n"
      "v_mov_b32 v129, s23\n"
      "s_add_u32 s41, s40, 0x2000\n"
      "buffer_store_dwordx2 v[128:129], v124, s[24:27], s41 offen sc1\n"
      "v_mov_b32 v126, v119\n"
      ".Lfk_cs39:\n"
      "buffer_store_dwordx2 v[126:127], v124, s[24:27], s40 offen sc1\n"
      ".Lfk_oe38:\n"
      "s_branch .Lfk_X27\n"
      ".Lfk_ort37:\n"
      "s_sub_u32 s59, s59, 1\n"
      "s_cbranch_scc1 .Lfk_od36\n"
      "s_sleep 2\n"
      "s_branch .Lfk_og35\n"
      "s_branch .Lfkpf0a\n"
      ".p2align 12\n"
      ".Lfk_F26:\n"
      ".Lfkpf0a:\n"
      "s_cbranch_execz .Lfkpx0b\n"
      "s_lshl_b32 s40, s39, 12\n"
      "s_add_u32 s51, s40, 36864\n"
      "s_add_u32 s53, s40, 94208\n"
      "s_lshl_b32 s40, s82, 7\n"
      "s_add_u32 s52, s53, s40\n"
      "s_add_u32 s53, s53, 3584\n"
      "s_cmp_eq_u32 s39, 7\n"
      "s_cbranch_scc1 .Lfk_f741\n"
      "s_sleep 5\n"
      "s_mov_b32 s28, s14\n"
      "s_and_b32 s29, s15, 0xffff\n"
      "s_mov_b32 s30, 1605632\n"
      "s_mov_b32 s31, 0x20000\n"
      "s_mul_i32 s40, s39, 229376\n"
      "s_lshl_b32 s41, s82, 7\n"
      "s_add_u32 s40, s40, s41\n"
      "s_mul_i32 s41, s39, 8192\n"
      "s_add_u32 m0, s41, 36864\n"
      "v_mul_u32_u24 v10, 3584, v11\n"
      "v_add_u32 v14, v10, v12\n"
      "v_add_u32 v15, v10, v13\n"
      "s_add_u32 s40, s40, 114688\n"
      "buffer_load_dwordx4 v14, s[28:31], s40 offen nt lds\n"
      "s_add_u32 m0, m0, 0x400\n"
      "s_add_u32 s40, s40, 28672\n"
      "buffer_load_dwordx4 v15, s[28:31], s40 offen nt lds\n"
      "s_add_u32 m0, m0, 0x400\n"
      "s_add_u32 s40, s40, 28672\n"
      "buffer_load_dwordx4 v14, s[28:31], s40 offen nt lds\n"
      "s_add_u32 m0, m0, 0x400\n"
      "s_add_u32 s40, s40, 28672\n"
      "buffer_load_dwordx4 v15, s[28:31], s40 offen nt lds\n"
      ".Lfkpf0f:\n"
      "s_cbranch_execz .Lfkpx1\n"
      "s_mov_b32 s28, s16\n"
      "s_and_b32 s29, s17, 0xffff\n"
      "s_mov_b32 s30, 1605632\n"
      "s_mov_b32 s31, 0x20000\n"
      "s_mul_i32 s40, s82, 57344\n"
      "s_lshl_b32 s41, s39, 7\n"
      "s_add_u32 s40, s40, s41\n"
      "s_mul_i32 s41, s39, 8192\n"
      "s_add_u32 m0, s41, 98304\n"
      "v_mul_u32_u24 v10, 896, v11\n"
      "v_add_u32 v14, v10, v12\n"
      "v_add_u32 v15, v10, v13\n"
      "buffer_load_dwordx4 v14, s[28:31], s40 offen nt lds\n"
      "s_add_u32 m0, m0, 0x400\n"
      "s_add_u32 s40, s40, 7168\n"
      "buffer_load_dwordx4 v15, s[28:31], s40 offen nt lds\n"
      "s_add_u32 m0, m0, 0x400\n"
      "s_add_u32 s40, s40, 7168\n"
      "buffer_load_dwordx4 v14, s[28:31], s40 offen nt lds\n"
      "s_add_u32 m0, m0, 0x400\n"
      "s_add_u32 s40, s40, 7168\n"
      "buffer_load_dwordx4 v15, s[28:31], s40 offen nt lds\n"
      "s_add_u32 m0, m0, 0x400\n"
      "s_add_u32 s40, s40, 7168\n"
      "buffer_load_dwordx4 v14, s[28:31], s40 offen nt lds\n"
      "s_add_u32 m0, m0, 0x400\n"
      "s_add_u32 s40, s40, 7168\n"
      "buffer_load_dwordx4 v15, s[28:31], s40 offen nt lds\n"
      "s_add_u32 m0, m0, 0x400\n"
      "s_add_u32 s40, s40, 7168\n"
      "buffer_load_dwordx4 v14, s[28:31], s40 offen nt lds\n"
      "s_add_u32 m0, m0, 0x400\n"
      "s_add_u32 s40, s40, 7168\n"
      "buffer_load_dwordx4 v15, s[28:31], s40 offen nt lds\n"
      "v_mov_b32 v146, v14\n"
      "v_mov_b32 v147, v15\n"
      "s_add_u32 s54, s40, 7168\n"
      "s_add_u32 s55, m0, 0x400\n"
      "s_lshl_b32 s40, s39, 13\n"
      "v_add_u32 v124, s40, v100\n"
      "v_mov_b32 v120, 0x3f803f80\n"
      "v_add_u32 v125, s40, v101\n"
      "v_mov_b32 v121, 0x3f803f80\n"
      "v_add_u32 v126, s40, v102\n"
      "v_mov_b32 v122, 0x3f803f80\n"
      "v_add_u32 v127, s40, v103\n"
      "v_mov_b32 v123, 0x3f803f80\n"
      "s_waitcnt vmcnt(8)\n"
      "ds_read_b64_tr_b16 v[4:5], v124 offset:32768\n"
      "ds_read_b64_tr_b16 v[6:7], v124 offset:33280\n"
      "ds_read_b64_tr_b16 v[8:9], v125 offset:32768\n"
      "ds_read_b64_tr_b16 v[10:11], v125 offset:33280\n"
      "ds_read_b64_tr_b16 v[12:13], v126 offset:32768\n"
      "ds_read_b64_tr_b16 v[14:15], v126 offset:33280\n"
      "ds_read_b64_tr_b16 v[16:17], v127 offset:32768\n"
      "ds_read_b64_tr_b16 v[18:19], v127 offset:33280\n"
      "ds_read_b64_tr_b16 v[20:21], v124 offset:36864\n"
      "ds_read_b64_tr_b16 v[22:23], v124 offset:37376\n"
      "ds_read_b64_tr_b16 v[24:25], v125 offset:36864\n"
      "ds_read_b64_tr_b16 v[26:27], v125 offset:37376\n"
      "ds_read_b64_tr_b16 v[28:29], v126 offset:36864\n"
      "ds_read_b64_tr_b16 v[30:31], v126 offset:37376\n"
      "ds_read_b64_tr_b16 v[32:33], v127 offset:36864\n"
      "ds_read_b64_tr_b16 v[34:35], v127 offset:37376\n"
      "s_waitcnt lgkmcnt(0)\n"
      "v_mfma_f32_16x16x32_bf16 v[148:151], v[120:123], v[4:7], 0\n"
      "v_mfma_f32_16x16x32_bf16 v[152:155], v[120:123], v[8:11], 0\n"
      "v_mfma_f32_16x16x32_bf16 v[156:159], v[120:123], v[12:15], 0\n"
      "v_mfma_f32_16x16x32_bf16 v[160:163], v[120:123], v[16:19], 0\n"
      "v_mfma_f32_16x16x32_bf16 v[148:151], v[120:123], v[20:23], v[148:151]\n"
      "v_mfma_f32_16x16x32_bf16 v[152:155], v[120:123], v[24:27], v[152:155]\n"
      "v_mfma_f32_16x16x32_bf16 v[156:159], v[120:123], v[28:31], v[156:159]\n"
      "v_mfma_f32_16x16x32_bf16 v[160:163], v[120:123], v[32:35], v[160:163]\n"
      "s_nop 7\n"
      "v_cndmask_b32 v128, v148, v152, s[60:61]\n"
      "v_cndmask_b32 v129, v156, v160, s[60:61]\n"
      "v_cndmask_b32 v128, v128, v129, s[62:63]\n"
      "ds_write_b32 v108, v128 offset:14336\n"
      ".Lfkpf0b:\n"
      "s_cbranch_execz .Lfkpx1b\n"
      "s_waitcnt lgkmcnt(0)\n"
      "s_barrier\n"
      "s_mov_b32 s32, s4\n"
      "s_and_b32 s33, s5, 0xffff\n"
      "s_mov_b32 s34, 896\n"
      "s_mov_b32 s35, 0x20000\n"
      "buffer_load_ushort v117, v104, s[32:35], 0 offen\n"
      "ds_read2st64_b32 v[128:129], v109 offset0:56 offset1:57\n"
      "ds_read2st64_b32 v[130:131], v109 offset0:58 offset1:59\n"
      "ds_read2st64_b32 v[132:133], v109 offset0:60 offset1:61\n"
      "ds_read_b32 v134, v109 offset:15872\n"
      "v_mov_b32 v135, 1472\n"
      "ds_read_b32 v135, v135\n"
      "s_waitcnt lgkmcnt(0)\n"
      "v_readfirstlane_b32 s90, v135\n"
      "v_pk_add_f32 v[128:129], v[128:129], v[130:131]\n"
      "v_pk_add_f32 v[128:129], v[128:129], v[132:133]\n"
      "v_add_f32 v128, v128, v129\n"
      "v_add_f32 v118, v128, v134\n"
      "v_add_u32 v100, 98304, v124\n"
      "v_add_u32 v101, 98304, v125\n"
      "v_add_u32 v102, 98304, v126\n"
      "v_add_u32 v103, 98304, v127\n"
      ".Lfkpf0c:\n"
      "s_cbranch_execz .Lfkpx2\n"
      "s_waitcnt vmcnt(0)\n"
      "v_lshlrev_b32 v117, 16, v117\n"
      "s_lshl_b32 s40, 1, s39\n"
      "v_mov_b32 v144, 0\n"
      "v_mov_b32 v145, s40\n"
      "s_add_u32 s91, s90, 120\n"
      ".Lfk_un42:\n"
      "s_memrealtime s[92:93]\n"
      "s_waitcnt lgkmcnt(0)\n"
      "s_sub_u32 s92, s92, s91\n"
      "s_cmp_gt_u32 s92, 4294965295\n"
      "s_cbranch_scc1 .Lfk_un42\n"
      "buffer_load_dwordx4 v[148:151], v1, s[24:27], s51 offen offset:0 sc1\n"
      "buffer_load_dwordx4 v[152:155], v1, s[24:27], s51 offen offset:1024 sc1\n"
      "buffer_load_dwordx4 v[156:159], v1, s[24:27], s51 offen offset:2048 sc1\n"
      "buffer_load_dwordx4 v[160:163], v1, s[24:27], s51 offen offset:3072 sc1\n"
      "ds_read_b64_tr_b16 v[36:37], v100 offset:0\n"
      "ds_read_b64_tr_b16 v[38:39], v100 offset:512\n"
      "ds_read_b64_tr_b16 v[40:41], v101 offset:0\n"
      "ds_read_b64_tr_b16 v[42:43], v101 offset:512\n"
      "ds_read_b64_tr_b16 v[44:45], v102 offset:0\n"
      "ds_read_b64_tr_b16 v[46:47], v102 offset:512\n"
      "ds_read_b64_tr_b16 v[48:49], v103 offset:0\n"
      "ds_read_b64_tr_b16 v[50:51], v103 offset:512\n"
      "ds_read_b64_tr_b16 v[52:53], v100 offset:4096\n"
      "ds_read_b64_tr_b16 v[54:55], v100 offset:4608\n"
      "ds_read_b64_tr_b16 v[56:57], v101 offset:4096\n"
      "ds_read_b64_tr_b16 v[58:59], v101 offset:4608\n"
      "ds_read_b64_tr_b16 v[60:61], v102 offset:4096\n"
      "ds_read_b64_tr_b16 v[62:63], v102 offset:4608\n"
      "ds_read_b64_tr_b16 v[64:65], v103 offset:4096\n"
      "ds_read_b64_tr_b16 v[66:67], v103 offset:4608\n"
      "s_mov_b32 s59, 65536\n"
      ".Lfk_fg43:\n"
      "s_waitcnt vmcnt(3)\n"
      "v_lshlrev_b32 v128, 16, v148\n"
      "v_lshlrev_b32 v132, 16, v150\n"
      "v_and_b32 v129, 0xffff0000, v148\n"
      "v_and_b32 v133, 0xffff0000, v150\n"
      "v_lshlrev_b32 v130, 16, v149\n"
      "v_lshlrev_b32 v134, 16, v151\n"
      "v_and_b32 v131, 0xffff0000, v149\n"
      "v_and_b32 v135, 0xffff0000, v151\n"
      "s_waitcnt vmcnt(2)\n"
      "v_dot2c_f32_bf16 v128, s76, v152\n"
      "v_dot2c_f32_bf16 v132, s76, v154\n"
      "v_dot2c_f32_bf16 v129, s77, v152\n"
      "v_dot2c_f32_bf16 v133, s77, v154\n"
      "v_dot2c_f32_bf16 v130, s76, v153\n"
      "v_dot2c_f32_bf16 v134, s76, v155\n"
      "v_dot2c_f32_bf16 v131, s77, v153\n"
      "v_dot2c_f32_bf16 v135, s77, v155\n"
      "s_waitcnt vmcnt(1)\n"
      "v_dot2c_f32_bf16 v128, s76, v156\n"
      "v_dot2c_f32_bf16 v132, s76, v158\n"
      "v_dot2c_f32_bf16 v129, s77, v156\n"
      "v_dot2c_f32_bf16 v133, s77, v158\n"
      "v_dot2c_f32_bf16 v130, s76, v157\n"
      "v_dot2c_f32_bf16 v134, s76, v159\n"
      "v_dot2c_f32_bf16 v131, s77, v157\n"
      "v_dot2c_f32_bf16 v135, s77, v159\n"
      "s_waitcnt vmcnt(0)\n"
      "s_mov_b64 exec, 1\n"
      "ds_or_b32 v144, v145 offset:1480\n"
      "s_mov_b64 exec, -1\n"
      "v_dot2c_f32_bf16 v128, s76, v160\n"
      "v_dot2c_f32_bf16 v132, s76, v162\n"
      "v_dot2c_f32_bf16 v129, s77, v160\n"
      "v_dot2c_f32_bf16 v133, s77, v162\n"
      "v_dot2c_f32_bf16 v130, s76, v161\n"
      "v_dot2c_f32_bf16 v134, s76, v163\n"
      "v_dot2c_f32_bf16 v131, s77, v161\n"
      "v_dot2c_f32_bf16 v135, s77, v163\n"
      "v_permlane32_swap_b32 v128, v132\n"
      "v_permlane32_swap_b32 v129, v133\n"
      "v_permlane32_swap_b32 v130, v134\n"
      "v_permlane32_swap_b32 v131, v135\n"
      "v_add_f32 v128, v128, v132\n"
      "v_add_f32 v130, v130, v134\n"
      "v_add_f32 v129, v129, v133\n"
      "v_add_f32 v131, v131, v135\n"
      "v_permlane16_swap_b32 v128, v130\n"
      "v_max3_u32 v138, v148, v149, v150\n"
      "v_permlane16_swap_b32 v129, v131\n"
      "v_add_f32 v128, v128, v130\n"
      "v_add_f32 v129, v129, v131\n"
      "v_cndmask_b32 v130, v129, v128, s[64:65]\n"
      "v_cndmask_b32 v131, v128, v129, s[64:65]\n"
      "v_add_f32 v131, v131, v117\n"
      "v_add_f32_dpp v140, v130, v131 row_ror:8 row_mask:0xf bank_mask:0xf\n"
      "v_cvt_pk_bf16_f32 v141, v140, v140\n"
      "ds_write_b16 v104, v141 offset:10240\n"
      "ds_read_b128 v[128:131], v105 offset:10240\n"
      "ds_read_b128 v[132:135], v105 offset:10304\n"
      "v_max3_u32 v139, v151, v152, v153\n"
      "v_max3_u32 v138, v138, v154, v155\n"
      "v_max3_u32 v139, v139, v156, v157\n"
      "v_max3_u32 v138, v138, v158, v159\n"
      "v_max3_u32 v139, v139, v160, v161\n"
      "v_max3_u32 v138, v138, v162, v163\n"
      "v_max_u32 v138, v138, v139\n"
      "v_cmp_eq_u32 vcc, -1, v138\n"
      "v_lshlrev_b32 v142, 16, v141\n"
      "s_cbranch_vccnz .Lfk_rt45\n"
      ".Lfk_fd44:\n"
      "v_mul_f32 v143, v142, v142\n"
      ".Lfkpf0d:\n"
      "s_cbranch_execz .Lfkpo1a\n"
      "s_waitcnt lgkmcnt(1)\n"
      "v_mfma_f32_16x16x32_bf16 v[148:151], v[128:131], v[4:7], 0\n"
      "v_add_f32_dpp v142, v142, v142 row_ror:8 row_mask:0xf bank_mask:0xf\n"
      "v_add_f32_dpp v143, v143, v143 row_ror:8 row_mask:0xf bank_mask:0xf\n"
      "v_mfma_f32_16x16x32_bf16 v[152:155], v[128:131], v[8:11], 0\n"
      "v_add_f32_dpp v142, v142, v142 row_ror:4 row_mask:0xf bank_mask:0xf\n"
      "v_add_f32_dpp v143, v143, v143 row_ror:4 row_mask:0xf bank_mask:0xf\n"
      "v_mfma_f32_16x16x32_bf16 v[156:159], v[128:131], v[12:15], 0\n"
      "v_add_f32_dpp v142, v142, v142 row_ror:2 row_mask:0xf bank_mask:0xf\n"
      "v_add_f32_dpp v143, v143, v143 row_ror:2 row_mask:0xf bank_mask:0xf\n"
      "v_mfma_f32_16x16x32_bf16 v[160:163], v[128:131], v[16:19], 0\n"
      "v_add_f32_dpp v142, v142, v142 row_ror:1 row_mask:0xf bank_mask:0xf\n"
      "v_add_f32_dpp v143, v143, v143 row_ror:1 row_mask:0xf bank_mask:0xf\n"
      "s_waitcnt lgkmcnt(0)\n"
      "v_mfma_f32_16x16x32_bf16 v[148:151], v[132:135], v[20:23], v[148:151]\n"
      "v_mov_b32 v144, v142\n"
      "v_mov_b32 v145, v143\n"
      "v_mfma_f32_16x16x32_bf16 v[152:155], v[132:135], v[24:27], v[152:155]\n"
      "v_permlane16_swap_b32 v142, v144\n"
      "v_permlane16_swap_b32 v143, v145\n"
      "v_add_f32 v142, v142, v144\n"
      "v_add_f32 v143, v143, v145\n"
      "v_mfma_f32_16x16x32_bf16 v[156:159], v[132:135], v[28:31], v[156:159]\n"
      "v_mov_b32 v144, v142\n"
      "v_mov_b32 v145, v143\n"
      "v_mfma_f32_16x16x32_bf16 v[160:163], v[132:135], v[32:35], v[160:163]\n"
      "v_permlane32_swap_b32 v142, v144\n"
      "v_permlane32_swap_b32 v143, v145\n"
      "v_add_f32 v142, v142, v144\n"
      "v_add_f32 v143, v143, v145\n"
      "s_lshl_b32 s40, s39, 3\n"
      "v_mov_b32 v144, s40\n"
      "s_mov_b64 exec, 1\n"
      "ds_write_b64 v144, v[142:143] offset:1344\n"
      "s_mov_b64 exec, -1\n"
      "s_cmp_eq_u32 s82, 0\n"
      "s_cbranch_scc0 .Lfk_nx46\n"
      "s_lshl_b32 s40, s39, 7\n"
      "v_and_b32 v120, 0x7c, v109\n"
      "v_add_u32 v120, s40, v120\n"
      "ds_read_b32 v120, v120 offset:10240\n"
      "s_waitcnt lgkmcnt(0)\n"
      "v_min_u32 v120, -2, v120\n"
      "buffer_store_dword v120, v112, s[24:27], s53 offen sc1\n"
      ".Lfk_nx46:\n"
      "v_cndmask_b32 v128, v148, v152, s[60:61]\n"
      "v_cndmask_b32 v129, v156, v160, s[60:61]\n"
      "v_cndmask_b32 v128, v128, v129, s[62:63]\n"
      "ds_write_b32 v108, v128 offset:12288\n"
      ".Lfkpf0e:\n"
      "s_cbranch_execz .Lfkpo1c\n"
      "s_waitcnt lgkmcnt(0)\n"
      "s_barrier\n"
      "v_mov_b32 v120, 0\n"
      "ds_read_b128 v[124:127], v120 offset:1344\n"
      "ds_read_b128 v[128:131], v120 offset:1360\n"
      "ds_read_b128 v[132:135], v120 offset:1376\n"
      "ds_read_b64 v[136:137], v120 offset:1392\n"
      "ds_read2st64_b32 v[138:139], v109 offset0:48 offset1:49\n"
      "ds_read2st64_b32 v[140:141], v109 offset0:50 offset1:51\n"
      "ds_read2st64_b32 v[142:143], v109 offset0:52 offset1:53\n"
      "ds_read_b32 v144, v109 offset:13824\n"
      "s_waitcnt lgkmcnt(4)\n"
      "v_pk_add_f32 v[124:125], v[124:125], v[126:127]\n"
      "v_pk_add_f32 v[128:129], v[128:129], v[130:131]\n"
      "v_pk_add_f32 v[132:133], v[132:133], v[134:135]\n"
      "v_pk_add_f32 v[124:125], v[124:125], v[128:129]\n"
      "v_pk_add_f32 v[132:133], v[132:133], v[136:137]\n"
      "v_pk_add_f32 v[124:125], v[124:125], v[132:133]\n"
      "v_mul_f32 v124, s78, v124\n"
      "s_waitcnt lgkmcnt(0)\n"
      "v_pk_add_f32 v[138:139], v[138:139], v[140:141]\n"
      "v_mul_f32 v125, s78, v125\n"
      "v_fma_f32 v125, -v124, v124, v125\n"
      "v_max_f32 v125, 0, v125\n"
      "v_add_f32 v125, s79, v125\n"
      "v_rsq_f32 v125, v125\n"
      "v_pk_add_f32 v[138:139], v[138:139], v[142:143]\n"
      "v_add_f32 v138, v138, v144\n"
      "v_add_f32 v138, v138, v139\n"
      "v_fma_f32 v138, -v124, v118, v138\n"
      "v_mul_f32 v138, v138, v125\n"
      "v_mul_f32 v139, s80, v138\n"
      "v_exp_f32 v139, v139\n"
      "s_nop 0\n"
      "v_add_f32 v139, 1.0, v139\n"
      "v_rcp_f32 v139, v139\n"
      "s_nop 0\n"
      "v_mul_f32 v138, v138, v139\n"
      "v_cvt_pk_bf16_f32 v138, v138, v138\n"
      "ds_write_b16 v106, v138 offset:11264\n"
      "ds_read_b128 v[128:131], v105 offset:11264\n"
      "ds_read_b128 v[132:135], v105 offset:11328\n"
      "s_waitcnt lgkmcnt(1)\n"
      "v_mfma_f32_16x16x32_bf16 v[148:151], v[128:131], v[36:39], 0\n"
      "v_mfma_f32_16x16x32_bf16 v[152:155], v[128:131], v[40:43], 0\n"
      "v_mfma_f32_16x16x32_bf16 v[156:159], v[128:131], v[44:47], 0\n"
      "v_mfma_f32_16x16x32_bf16 v[160:163], v[128:131], v[48:51], 0\n"
      "s_waitcnt lgkmcnt(0)\n"
      "v_mfma_f32_16x16x32_bf16 v[148:151], v[132:135], v[52:55], v[148:151]\n"
      "v_mfma_f32_16x16x32_bf16 v[152:155], v[132:135], v[56:59], v[152:155]\n"
      "v_mfma_f32_16x16x32_bf16 v[156:159], v[132:135], v[60:63], v[156:159]\n"
      "v_mfma_f32_16x16x32_bf16 v[160:163], v[132:135], v[64:67], v[160:163]\n"
      "s_nop 5\n"
      "v_cndmask_b32 v128, v148, v152, s[60:61]\n"
      "s_nop 0\n"
      "v_cndmask_b32 v129, v156, v160, s[60:61]\n"
      "v_cndmask_b32 v128, v128, v129, s[62:63]\n"
      "s_nop 1\n"
      "v_mov_b32_dpp v129, v128 quad_perm:[1,0,3,2] row_mask:0xf bank_mask:0xf\n"
      "v_cvt_pk_bf16_f32 v128, v128, v129\n"
      "v_min_u32 v128, -2, v128\n"
      "buffer_store_dword v128, v110, s[24:27], s52 offen sc1\n"
      "s_branch .Lfk_X27\n"
      ".Lfk_f741:\n"
      "v_lshlrev_b32 v124, 7, v0\n"
      "s_lshl_b32 s50, s36, 10\n"
      "s_add_u32 s50, s50, 1110016\n"
      "s_mov_b32 s59, 65536\n"
      "s_mov_b32 exec_lo, 0xff\n"
      "s_mov_b32 exec_hi, 0\n"
      ".Lfk_ls47:\n"
      "buffer_load_dwordx3 v[120:122], v124, s[24:27], s50 offen sc1\n"
      "s_waitcnt vmcnt(0)\n"
      "v_xor_b32 v120, s23, v120\n"
      "v_xor_b32 v122, s23, v122\n"
      "v_or_b32 v120, v120, v122\n"
      "v_cmp_ne_u32 vcc, 0, v120\n"
      "s_and_b64 exec, vcc, exec\n"
      "s_cbranch_scc0 .Lfk_ld48\n"
      "s_sleep 1\n"
      "s_sub_u32 s59, s59, 1\n"
      "s_cbranch_scc0 .Lfk_ls47\n"
      ".Lfk_ld48:\n"
      "s_mov_b64 exec, -1\n"
      "v_readlane_b32 s40, v121, 0\n"
      "s_nop 1\n"
      "v_subrev_u32 v121, s40, v121\n"
      "s_nop 1\n"
      "v_max_i32_dpp v121, v121, v121 quad_perm:[1,0,3,2] row_mask:0xf bank_mask:0xf\n"
      "s_nop 1\n"
      "v_max_i32_dpp v121, v121, v121 quad_perm:[2,3,0,1] row_mask:0xf bank_mask:0xf\n"
      "s_nop 0\n"
      "v_readlane_b32 s41, v121, 0\n"
      "v_readlane_b32 s42, v121, 4\n"
      "v_mov_b32 v124, 1472\n"
      "v_mov_b32 v120, s23\n"
      "v_mov_b32 v122, s23\n"
      "s_max_i32 s41, s41, s42\n"
      "s_add_u32 s90, s40, s41\n"
      "v_mov_b32 v121, s90\n"
      "v_mov_b32 v123, s90\n"
      "v_mov_b32 v125, 0\n"
      "ds_write2_b32 v124, v121, v125 offset0:0 offset1:2\n"
      "s_cmp_eq_u32 s37, 19\n"
      "s_cbranch_scc0 .Lfk_dc49\n"
      "s_lshl_b32 s40, s36, 7\n"
      "s_mov_b64 exec, 0xff\n"
      "buffer_store_dwordx4 v[120:123], v1, s[24:27], s40 offen offset:128 sc1\n"
      "s_mov_b64 exec, -1\n"
      ".Lfk_dc49:\n"
      "s_waitcnt lgkmcnt(0)\n"
      "s_barrier\n"
      "v_lshrrev_b32 v126, 3, v0\n"
      "v_and_b32 v125, 7, v0\n"
      "v_lshlrev_b32 v125, 4, v125\n"
      "v_lshl_or_b32 v126, v126, 12, v125\n"
      "s_lshl_b32 s40, s82, 7\n"
      "s_add_u32 s40, s40, 1118208\n"
      "s_mov_b32 s59, 1048576\n"
      ".Lfk_cn50:\n"
      "ds_read_b32 v125, v124 offset:8\n"
      "s_sub_u32 s59, s59, 1\n"
      "s_cbranch_scc1 .Lfk_cn51\n"
      "s_waitcnt lgkmcnt(0)\n"
      "v_cmp_ne_u32 vcc, 0x7f, v125\n"
      "s_cbranch_vccnz .Lfk_cn50\n"
      ".Lfk_cn51:\n"
      "s_memrealtime s[92:93]\n"
      "s_waitcnt lgkmcnt(0)\n"
      "v_mov_b32 v121, s92\n"
      "v_mov_b32 v123, s92\n"
      "buffer_store_dwordx4 v[120:123], v126, s[24:27], s40 offen sc1\n"
      "s_waitcnt lgkmcnt(0)\n"
      "s_barrier\n"
      "s_branch .Lfk_X27\n"
      ".Lfk_rt45:\n"
      "s_sub_u32 s59, s59, 1\n"
      "s_cbranch_scc1 .Lfk_fd44\n"
      "s_sleep 2\n"
      "s_waitcnt lgkmcnt(0)\n"
      "s_mov_b64 exec, vcc\n"
      "buffer_load_dwordx4 v[148:151], v1, s[24:27], s51 offen offset:0 sc1\n"
      "buffer_load_dwordx4 v[152:155], v1, s[24:27], s51 offen offset:1024 sc1\n"
      "buffer_load_dwordx4 v[156:159], v1, s[24:27], s51 offen offset:2048 sc1\n"
      "buffer_load_dwordx4 v[160:163], v1, s[24:27], s51 offen offset:3072 sc1\n"
      "s_waitcnt vmcnt(0)\n"
      "s_mov_b64 exec, -1\n"
      "s_branch .Lfk_fg43\n"
      "s_branch .Lfkpx0\n"
      ".p2align 12\n"
      ".Lfk_X27:\n"
      ".Lfkpx0:\n"
      "s_cbranch_execz .Lfkpf1a\n"
      "s_cmp_eq_u32 s37, 20\n"
      "s_cbranch_scc0 .Lfk_no52\n"
      "s_mov_b32 s32, s18\n"
      "s_and_b32 s33, s19, 0xffff\n"
      "s_mov_b32 s34, 896\n"
      "s_mov_b32 s35, 0x20000\n"
      "buffer_load_dword v3, v109, s[32:35], 0 offen\n"
      ".Lfk_no52:\n"
      "s_lshl_b32 s40, s39, 10\n"
      "s_add_u32 s52, s40, s38\n"
      "s_add_u32 s52, s52, 90112\n"
      "s_lshl_b32 s40, s37, 8\n"
      "s_add_u32 s53, s40, s38\n"
      "s_add_u32 s53, s53, 90112\n"
      "s_lshr_b32 s40, s39, 2\n"
      "s_mul_i32 s40, s40, 576\n"
      "s_and_b32 s41, s39, 3\n"
      "s_lshl_b32 s41, s41, 7\n"
      "s_add_u32 s40, s40, s41\n"
      "v_and_b32 v146, 15, v0\n"
      "v_lshrrev_b32 v147, 4, v0\n"
      "v_lshlrev_b32 v146, 3, v146\n"
      "v_lshl_or_b32 v146, v147, 1, v146\n"
      "v_add_u32 v146, s40, v146\n"
      "s_cmp_eq_u32 s87, s89\n"
      "s_cbranch_scc0 .Lfk_xt55\n"
      "s_sleep 4\n"
      "s_and_b32 s40, s88, s94\n"
      "s_lshl_b32 s40, s40, 4\n"
      "s_add_u32 s40, s40, s37\n"
      "s_lshr_b32 s40, s40, 2\n"
      "s_lshl_b32 s40, s40, 11\n"
      "s_add_u32 s40, s40, s38\n"
      "s_add_u32 s50, s40, 98304\n"
      "v_lshrrev_b32 v120, 4, v0\n"
      "v_and_b32 v121, 15, v0\n"
      "v_lshlrev_b32 v121, 4, v121\n"
      "v_lshl_or_b32 v120, v120, 9, v121\n"
      "buffer_load_dwordx4 v[124:127], v120, s[24:27], s50 offen sc1\n"
      "buffer_load_dwordx4 v[128:131], v120, s[24:27], s50 offen offset:256 sc1\n"
      "s_mov_b32 s59, 65536\n"
      ".Lfk_cs56:\n"
      "s_waitcnt vmcnt(0)\n"
      "v_xor_b32 v132, s23, v125\n"
      "v_xor_b32 v133, s23, v127\n"
      "v_xor_b32 v134, s23, v129\n"
      "v_xor_b32 v135, s23, v131\n"
      "v_or3_b32 v132, v132, v133, v134\n"
      "v_or_b32 v132, v132, v135\n"
      "v_cmp_ne_u32 vcc, 0, v132\n"
      "s_cbranch_vccnz .Lfk_rt58\n"
      ".Lfk_cs57:\n"
      "v_permlane32_swap_b32 v124, v128\n"
      "v_permlane32_swap_b32 v126, v130\n"
      "v_add_f32 v124, v124, v128\n"
      "v_add_f32 v126, v126, v130\n"
      "s_nop 1\n"
      "v_permlane16_swap_b32 v124, v126\n"
      "v_add_f32 v124, v124, v126\n"
      "v_mul_f32 v118, s78, v124\n"
      ".Lfk_xt55:\n"
      ".Lfkpx0b:\n"
      "s_cbranch_execz .Lfkpf1d\n"
      "s_xor_b32 s40, s89, 1\n"
      "s_and_b32 s40, s40, 3\n"
      "s_lshl_b32 s40, s40, 1\n"
      "s_cmp_eq_u32 s86, 2\n"
      "s_cselect_b32 s40, 7, s40\n"
      "s_cmp_eq_u32 s39, s40\n"
      "s_cbranch_scc0 .Lfk_ht59\n"
      "s_cmp_eq_u32 s86, 2\n"
      "s_cbranch_scc1 .Lfk_ht60\n"
      "s_lshl_b32 s50, s36, 7\n"
      "v_mov_b32 v125, 0\n"
      "s_mov_b32 s59, 65536\n"
      ".Lfk_t61:\n"
      "buffer_load_dwordx3 v[122:124], v125, s[24:27], s50 offen offset:128 sc1\n"
      "s_waitcnt vmcnt(0)\n"
      "v_xor_b32 v122, s23, v122\n"
      "v_xor_b32 v124, s23, v124\n"
      "v_or_b32 v122, v122, v124\n"
      "v_cmp_ne_u32 vcc, 0, v122\n"
      "s_cbranch_vccz .Lfk_t62\n"
      "s_sub_u32 s59, s59, 1\n"
      "s_sleep 1\n"
      "s_cbranch_scc0 .Lfk_t61\n"
      ".Lfk_t62:\n"
      "v_readfirstlane_b32 s90, v123\n"
      "s_nop 1\n"
      ".Lfk_ht60:\n"
      "s_add_u32 s91, s90, 150\n"
      ".Lfk_un63:\n"
      "s_memrealtime s[92:93]\n"
      "s_waitcnt lgkmcnt(0)\n"
      "s_sub_u32 s92, s92, s91\n"
      "s_cmp_gt_u32 s92, 4294965295\n"
      "s_cbranch_scc1 .Lfk_un63\n"
      "v_lshlrev_b32 v124, 7, v0\n"
      "s_lshl_b32 s50, s36, 12\n"
      "s_add_u32 s50, s50, 1118208\n"
      "s_mov_b32 s59, 65536\n"
      "s_mov_b32 exec_lo, 0xfffffff\n"
      "s_mov_b32 exec_hi, 0\n"
      ".Lfk_ls64:\n"
      "buffer_load_dwordx3 v[120:122], v124, s[24:27], s50 offen sc1\n"
      "s_waitcnt vmcnt(0)\n"
      "v_xor_b32 v120, s23, v120\n"
      "v_xor_b32 v122, s23, v122\n"
      "v_or_b32 v120, v120, v122\n"
      "v_cmp_ne_u32 vcc, 0, v120\n"
      "s_and_b64 exec, vcc, exec\n"
      "s_cbranch_scc0 .Lfk_ld65\n"
      "s_sleep 1\n"
      "s_sub_u32 s59, s59, 1\n"
      "s_cbranch_scc0 .Lfk_ls64\n"
      ".Lfk_ld65:\n"
      "s_mov_b64 exec, -1\n"
      "v_readlane_b32 s40, v121, 0\n"
      "v_cmp_gt_u32 vcc, 28, v0\n"
      "s_nop 0\n"
      "v_subrev_u32 v121, s40, v121\n"
      "v_cndmask_b32 v121, 0, v121, vcc\n"
      "s_nop 1\n"
      "v_max_i32_dpp v121, v121, v121 row_ror:8 row_mask:0xf bank_mask:0xf\n"
      "s_nop 1\n"
      "v_max_i32_dpp v121, v121, v121 row_ror:4 row_mask:0xf bank_mask:0xf\n"
      "s_nop 1\n"
      "v_max_i32_dpp v121, v121, v121 row_ror:2 row_mask:0xf bank_mask:0xf\n"
      "s_nop 1\n"
      "v_max_i32_dpp v121, v121, v121 row_ror:1 row_mask:0xf bank_mask:0xf\n"
      "s_nop 0\n"
      "v_readlane_b32 s41, v121, 0\n"
      "v_readlane_b32 s42, v121, 16\n"
      "v_mov_b32 v124, 1472\n"
      "s_nop 1\n"
      "s_max_i32 s41, s41, s42\n"
      "s_add_u32 s40, s40, s41\n"
      "v_mov_b32 v121, s40\n"
      "ds_write_b32 v124, v121\n"
      ".Lfk_ht59:\n"
      "s_waitcnt lgkmcnt(0)\n"
      "s_barrier\n"
      "v_mov_b32 v124, 1472\n"
      "ds_read_b32 v124, v124\n"
      "s_waitcnt lgkmcnt(0)\n"
      "v_readfirstlane_b32 s90, v124\n"
      "s_cmp_eq_u32 s86, 2\n"
      "s_cbranch_scc1 .Lfk_xf54\n"
      "s_cmp_eq_u32 s87, s89\n"
      "s_cbranch_scc0 .Lfk_bx53\n"
      "s_min_u32 s95, s39, 6\n"
      "s_lshl_b32 s40, s95, 12\n"
      "s_add_u32 s51, s40, 94208\n"
      "s_add_u32 s91, s90, 84\n"
      ".Lfk_un66:\n"
      "s_memrealtime s[92:93]\n"
      "s_waitcnt lgkmcnt(0)\n"
      "s_sub_u32 s92, s92, s91\n"
      "s_cmp_gt_u32 s92, 4294965295\n"
      "s_cbranch_scc1 .Lfk_un66\n"
      "buffer_load_dwordx4 v[148:151], v1, s[24:27], s51 offen offset:0 sc1\n"
      "buffer_load_dwordx4 v[152:155], v1, s[24:27], s51 offen offset:1024 sc1\n"
      "buffer_load_dwordx4 v[156:159], v1, s[24:27], s51 offen offset:2048 sc1\n"
      "buffer_load_dwordx4 v[160:163], v113, s[24:27], s51 offen offset:3072 sc1\n"
      "s_mov_b32 s59, 65536\n"
      ".Lfk_cg67:\n"
      "s_waitcnt vmcnt(3)\n"
      "v_lshlrev_b32 v128, 16, v148\n"
      "v_lshlrev_b32 v132, 16, v150\n"
      "v_and_b32 v129, 0xffff0000, v148\n"
      "v_and_b32 v133, 0xffff0000, v150\n"
      "v_lshlrev_b32 v130, 16, v149\n"
      "v_lshlrev_b32 v134, 16, v151\n"
      "v_and_b32 v131, 0xffff0000, v149\n"
      "v_and_b32 v135, 0xffff0000, v151\n"
      "s_waitcnt vmcnt(2)\n"
      "v_dot2c_f32_bf16 v128, s76, v152\n"
      "v_dot2c_f32_bf16 v132, s76, v154\n"
      "v_dot2c_f32_bf16 v129, s77, v152\n"
      "v_dot2c_f32_bf16 v133, s77, v154\n"
      "v_dot2c_f32_bf16 v130, s76, v153\n"
      "v_dot2c_f32_bf16 v134, s76, v155\n"
      "v_dot2c_f32_bf16 v131, s77, v153\n"
      "v_dot2c_f32_bf16 v135, s77, v155\n"
      "s_waitcnt vmcnt(1)\n"
      "v_dot2c_f32_bf16 v128, s76, v156\n"
      "v_dot2c_f32_bf16 v132, s76, v158\n"
      "v_dot2c_f32_bf16 v129, s77, v156\n"
      "v_dot2c_f32_bf16 v133, s77, v158\n"
      "v_dot2c_f32_bf16 v130, s76, v157\n"
      "v_dot2c_f32_bf16 v134, s76, v159\n"
      "v_dot2c_f32_bf16 v131, s77, v157\n"
      "v_dot2c_f32_bf16 v135, s77, v159\n"
      "s_waitcnt vmcnt(0)\n"
      "v_dot2c_f32_bf16 v128, s76, v160\n"
      "v_dot2c_f32_bf16 v132, s76, v162\n"
      "v_dot2c_f32_bf16 v129, s77, v160\n"
      "v_dot2c_f32_bf16 v133, s77, v162\n"
      "v_dot2c_f32_bf16 v130, s76, v161\n"
      "v_dot2c_f32_bf16 v134, s76, v163\n"
      "v_dot2c_f32_bf16 v131, s77, v161\n"
      "v_dot2c_f32_bf16 v135, s77, v163\n"
      "v_permlane32_swap_b32 v128, v132\n"
      "v_permlane32_swap_b32 v129, v133\n"
      "v_permlane32_swap_b32 v130, v134\n"
      "v_permlane32_swap_b32 v131, v135\n"
      "v_add_f32 v128, v128, v132\n"
      "v_add_f32 v130, v130, v134\n"
      "v_add_f32 v129, v129, v133\n"
      "v_add_f32 v131, v131, v135\n"
      "v_permlane16_swap_b32 v128, v130\n"
      "v_max3_u32 v138, v148, v149, v150\n"
      "v_permlane16_swap_b32 v129, v131\n"
      "v_add_f32 v128, v128, v130\n"
      "v_add_f32 v129, v129, v131\n"
      "v_cndmask_b32 v130, v129, v128, s[64:65]\n"
      "v_cndmask_b32 v131, v128, v129, s[64:65]\n"
      "v_max3_u32 v139, v151, v152, v153\n"
      "v_add_f32_dpp v140, v130, v131 row_ror:8 row_mask:0xf bank_mask:0xf\n"
      "v_cvt_pk_bf16_f32 v141, v140, v140\n"
      "ds_write_b16 v104, v141 offset:10240\n"
      "ds_read_b128 v[128:131], v105 offset:10240\n"
      "ds_read_b128 v[132:135], v105 offset:10304\n"
      "v_max3_u32 v138, v138, v154, v155\n"
      "v_max3_u32 v139, v139, v156, v157\n"
      "v_max3_u32 v138, v138, v158, v159\n"
      "v_max3_u32 v139, v139, v160, v161\n"
      "v_max3_u32 v138, v138, v162, v163\n"
      "v_max_u32 v138, v138, v139\n"
      "v_cmp_eq_u32 vcc, -1, v138\n"
      "v_lshlrev_b32 v142, 16, v141\n"
      "s_cbranch_vccnz .Lfk_rt69\n"
      ".Lfk_cd68:\n"
      "v_mul_f32 v143, v142, v142\n"
      ".Lfkpx1:\n"
      "s_cbranch_execz .Lfkpf1e\n"
      "s_waitcnt lgkmcnt(1)\n"
      "v_mfma_f32_16x16x32_bf16 v[148:151], v[128:131], v[68:71], 0\n"
      "v_add_f32_dpp v142, v142, v142 row_ror:8 row_mask:0xf bank_mask:0xf\n"
      "v_add_f32_dpp v143, v143, v143 row_ror:8 row_mask:0xf bank_mask:0xf\n"
      "v_mfma_f32_16x16x32_bf16 v[152:155], v[128:131], v[72:75], 0\n"
      "v_add_f32_dpp v142, v142, v142 row_ror:4 row_mask:0xf bank_mask:0xf\n"
      "v_add_f32_dpp v143, v143, v143 row_ror:4 row_mask:0xf bank_mask:0xf\n"
      "v_mfma_f32_16x16x32_bf16 v[156:159], v[128:131], v[76:79], 0\n"
      "v_add_f32_dpp v142, v142, v142 row_ror:2 row_mask:0xf bank_mask:0xf\n"
      "v_add_f32_dpp v143, v143, v143 row_ror:2 row_mask:0xf bank_mask:0xf\n"
      "v_mfma_f32_16x16x32_bf16 v[160:163], v[128:131], v[80:83], 0\n"
      "v_add_f32_dpp v142, v142, v142 row_ror:1 row_mask:0xf bank_mask:0xf\n"
      "v_add_f32_dpp v143, v143, v143 row_ror:1 row_mask:0xf bank_mask:0xf\n"
      "s_waitcnt lgkmcnt(0)\n"
      "v_mfma_f32_16x16x32_bf16 v[148:151], v[132:135], v[84:87], v[148:151]\n"
      "v_mov_b32 v144, v142\n"
      "v_mov_b32 v145, v143\n"
      "v_mfma_f32_16x16x32_bf16 v[152:155], v[132:135], v[88:91], v[152:155]\n"
      "v_permlane16_swap_b32 v142, v144\n"
      "v_permlane16_swap_b32 v143, v145\n"
      "v_add_f32 v142, v142, v144\n"
      "v_add_f32 v143, v143, v145\n"
      "v_mfma_f32_16x16x32_bf16 v[156:159], v[132:135], v[92:95], v[156:159]\n"
      "v_mov_b32 v144, v142\n"
      "v_mov_b32 v145, v143\n"
      "v_mfma_f32_16x16x32_bf16 v[160:163], v[132:135], v[96:99], v[160:163]\n"
      "v_permlane32_swap_b32 v142, v144\n"
      "v_permlane32_swap_b32 v143, v145\n"
      "v_add_f32 v142, v142, v144\n"
      "v_add_f32 v143, v143, v145\n"
      "s_lshl_b32 s40, s88, 3\n"
      "v_mov_b32 v144, s40\n"
      "s_mov_b64 exec, 1\n"
      "ds_write_b64 v144, v[142:143] offset:1408\n"
      "s_mov_b64 exec, -1\n"
      "s_lshl_b32 s40, s88, 8\n"
      "v_add_u32 v120, s40, v109\n"
      "s_nop 0\n"
      "v_cndmask_b32 v128, v148, v152, s[60:61]\n"
      "v_cndmask_b32 v129, v156, v160, s[60:61]\n"
      "v_cndmask_b32 v128, v128, v129, s[62:63]\n"
      "v_fma_f32 v128, -v142, v118, v128\n"
      "ds_write_b32 v120, v128 offset:1536\n"
      "s_branch .Lfk_bx53\n"
      ".Lfk_xf54:\n"
      "s_cmp_eq_u32 s39, 7\n"
      "s_cbranch_scc1 .Lfk_bx53\n"
      "s_mov_b32 s95, s39\n"
      ".Lfkpx1b:\n"
      "s_cbranch_execz .Lfkpz0\n"
      "s_lshl_b32 s40, s95, 12\n"
      "s_add_u32 s51, s40, 94208\n"
      "s_add_u32 s91, s90, 124\n"
      ".Lfk_un70:\n"
      "s_memrealtime s[92:93]\n"
      "s_waitcnt lgkmcnt(0)\n"
      "s_sub_u32 s92, s92, s91\n"
      "s_cmp_gt_u32 s92, 4294965295\n"
      "s_cbranch_scc1 .Lfk_un70\n"
      "buffer_load_dwordx4 v[148:151], v1, s[24:27], s51 offen offset:0 sc1\n"
      "buffer_load_dwordx4 v[152:155], v1, s[24:27], s51 offen offset:1024 sc1\n"
      "buffer_load_dwordx4 v[156:159], v1, s[24:27], s51 offen offset:2048 sc1\n"
      "buffer_load_dwordx4 v[160:163], v113, s[24:27], s51 offen offset:3072 sc1\n"
      "s_mov_b32 s59, 65536\n"
      ".Lfk_cg71:\n"
      "s_waitcnt vmcnt(3)\n"
      "v_lshlrev_b32 v128, 16, v148\n"
      "v_lshlrev_b32 v132, 16, v150\n"
      "v_and_b32 v129, 0xffff0000, v148\n"
      "v_and_b32 v133, 0xffff0000, v150\n"
      "v_lshlrev_b32 v130, 16, v149\n"
      "v_lshlrev_b32 v134, 16, v151\n"
      "v_and_b32 v131, 0xffff0000, v149\n"
      "v_and_b32 v135, 0xffff0000, v151\n"
      "s_waitcnt vmcnt(2)\n"
      "v_dot2c_f32_bf16 v128, s76, v152\n"
      "v_dot2c_f32_bf16 v132, s76, v154\n"
      "v_dot2c_f32_bf16 v129, s77, v152\n"
      "v_dot2c_f32_bf16 v133, s77, v154\n"
      "v_dot2c_f32_bf16 v130, s76, v153\n"
      "v_dot2c_f32_bf16 v134, s76, v155\n"
      "v_dot2c_f32_bf16 v131, s77, v153\n"
      "v_dot2c_f32_bf16 v135, s77, v155\n"
      "s_waitcnt vmcnt(1)\n"
      "v_dot2c_f32_bf16 v128, s76, v156\n"
      "v_dot2c_f32_bf16 v132, s76, v158\n"
      "v_dot2c_f32_bf16 v129, s77, v156\n"
      "v_dot2c_f32_bf16 v133, s77, v158\n"
      "v_dot2c_f32_bf16 v130, s76, v157\n"
      "v_dot2c_f32_bf16 v134, s76, v159\n"
      "v_dot2c_f32_bf16 v131, s77, v157\n"
      "v_dot2c_f32_bf16 v135, s77, v159\n"
      "s_waitcnt vmcnt(0)\n"
      "v_dot2c_f32_bf16 v128, s76, v160\n"
      "v_dot2c_f32_bf16 v132, s76, v162\n"
      "v_dot2c_f32_bf16 v129, s77, v160\n"
      "v_dot2c_f32_bf16 v133, s77, v162\n"
      "v_dot2c_f32_bf16 v130, s76, v161\n"
      "v_dot2c_f32_bf16 v134, s76, v163\n"
      "v_dot2c_f32_bf16 v131, s77, v161\n"
      "v_dot2c_f32_bf16 v135, s77, v163\n"
      "v_permlane32_swap_b32 v128, v132\n"
      "v_permlane32_swap_b32 v129, v133\n"
      "v_permlane32_swap_b32 v130, v134\n"
      "v_permlane32_swap_b32 v131, v135\n"
      "v_add_f32 v128, v128, v132\n"
      "v_add_f32 v130, v130, v134\n"
      "v_add_f32 v129, v129, v133\n"
      "v_add_f32 v131, v131, v135\n"
      "v_permlane16_swap_b32 v128, v130\n"
      "v_max3_u32 v138, v148, v149, v150\n"
      "v_permlane16_swap_b32 v129, v131\n"
      "v_add_f32 v128, v128, v130\n"
      "v_add_f32 v129, v129, v131\n"
      "v_cndmask_b32 v130, v129, v128, s[64:65]\n"
      "v_cndmask_b32 v131, v128, v129, s[64:65]\n"
      "v_max3_u32 v139, v151, v152, v153\n"
      "v_add_f32_dpp v140, v130, v131 row_ror:8 row_mask:0xf bank_mask:0xf\n"
      "v_max3_u32 v138, v138, v154, v155\n"
      "v_max3_u32 v139, v139, v156, v157\n"
      "v_max3_u32 v138, v138, v158, v159\n"
      "v_max3_u32 v139, v139, v160, v161\n"
      "v_max3_u32 v138, v138, v162, v163\n"
      "v_max_u32 v138, v138, v139\n"
      "v_cmp_eq_u32 vcc, -1, v138\n"
      "v_cvt_pk_bf16_f32 v141, v140, v140\n"
      "s_cbranch_vccnz .Lfk_rt73\n"
      ".Lfk_cd72:\n"
      "v_lshlrev_b32 v117, 16, v141\n"
      ".Lfk_bx53:\n"
      ".Lfkpx2:\n"
      "s_cbranch_execz .Lfkpa1b\n"
      "s_waitcnt lgkmcnt(0)\n"
      "s_barrier\n"
      "s_cmp_eq_u32 s39, 0\n"
      "s_cbranch_scc0 .Lfk_q174\n"
      "s_cmp_eq_u32 s94, 2\n"
      "s_cbranch_scc1 .Lfk_q175\n"
      "ds_read2st64_b32 v[140:141], v109 offset0:6 offset1:7\n"
      "v_mov_b32 v120, 0\n"
      "ds_read_b128 v[124:127], v120 offset:1408\n"
      "v_mov_b32 v143, s23\n"
      "v_mov_b32 v145, s23\n"
      "s_cmp_eq_u32 s94, 1\n"
      "s_waitcnt lgkmcnt(1)\n"
      "s_cbranch_scc1 .Lfk_q376\n"
      "v_add_f32 v140, v140, v141\n"
      ".Lfk_q377:\n"
      "s_nop 1\n"
      "v_mov_b32_dpp v156, v140 quad_perm:[1,0,3,2] row_mask:0xf bank_mask:0xf\n"
      "v_cvt_pk_bf16_f32 v142, v140, v156\n"
      "buffer_store_dwordx2 v[142:143], v111, s[24:27], s53 offen sc1\n"
      "s_cmp_lt_u32 s37, 4\n"
      "s_cbranch_scc0 .Lfk_q175\n"
      "s_lshl_b32 s40, s37, 7\n"
      "s_add_u32 s40, s40, s38\n"
      "s_add_u32 s40, s40, 114688\n"
      "s_waitcnt lgkmcnt(0)\n"
      "v_add_f32 v128, v124, v126\n"
      "v_add_f32 v130, v125, v127\n"
      "v_mov_b32 v129, s23\n"
      "v_mov_b32 v131, s23\n"
      "s_mov_b64 exec, 0xff\n"
      "buffer_store_dwordx4 v[128:131], v1, s[24:27], s40 offen sc1\n"
      "s_mov_b64 exec, -1\n"
      "s_branch .Lfk_q175\n"
      ".Lfk_q174:\n"
      "s_cmp_eq_u32 s39, 7\n"
      "s_cselect_b32 s40, s2, 1\n"
      "s_cmp_eq_u32 s40, 0\n"
      "s_cbranch_scc0 .Lfk_ne79\n"
      "v_mov_b32 v120, s23\n"
      "v_mov_b32 v121, 0\n"
      "s_mov_b64 exec, 1\n"
      "buffer_store_dword v120, v121, s[24:27], 0 offen sc1\n"
      "s_mov_b64 exec, -1\n"
      ".Lfk_ne79:\n"
      ".Lfk_q175:\n"
      "s_memrealtime s[92:93]\n"
      "s_add_u32 s91, s90, 161\n"
      "s_waitcnt lgkmcnt(0)\n"
      "s_sub_u32 s92, s92, s91\n"
      "s_cmp_gt_i32 s92, 0\n"
      "s_cselect_b32 s93, 14, 0\n"
      "s_max_i32 s92, s92, 0\n"
      "s_add_u32 s90, s90, s92\n"
      "s_add_u32 s90, s90, s93\n"
      "s_add_u32 s91, s90, 179\n"
      ".Lfk_un80:\n"
      "s_memrealtime s[92:93]\n"
      "s_waitcnt lgkmcnt(0)\n"
      "s_sub_u32 s92, s92, s91\n"
      "s_cmp_gt_u32 s92, 4294965295\n"
      "s_cbranch_scc1 .Lfk_un80\n"
      "buffer_load_dwordx4 v[120:123], v1, s[24:27], s52 offen sc1\n"
      "s_mov_b32 s59, 65536\n"
      ".Lfk_qg81:\n"
      "s_waitcnt vmcnt(0)\n"
      "v_lshlrev_b32 v124, 16, v120\n"
      "v_lshlrev_b32 v125, 16, v122\n"
      "v_and_b32 v126, 0xffff0000, v120\n"
      "v_and_b32 v127, 0xffff0000, v122\n"
      "v_cmp_ne_u32 vcc, s23, v121\n"
      "v_cmp_ne_u32 s[56:57], s23, v123\n"
      "v_permlane32_swap_b32 v124, v125\n"
      "v_permlane32_swap_b32 v126, v127\n"
      "v_add_f32 v124, v124, v125\n"
      "v_add_f32 v126, v126, v127\n"
      "s_nop 1\n"
      "v_permlane16_swap_b32 v124, v126\n"
      "v_add_f32 v124, v124, v126\n"
      "v_cvt_pk_bf16_f32 v124, v124, v124\n"
      "ds_write_b16 v146, v124 offset:0\n"
      "s_or_b64 vcc, vcc, s[56:57]\n"
      "s_cbranch_vccnz .Lfk_rt83\n"
      ".Lfk_qd82:\n"
      "s_waitcnt lgkmcnt(0)\n"
      "s_barrier\n"
      "s_lshl_b32 s50, s83, 13\n"
      "s_lshl_b32 s40, s39, 11\n"
      "s_add_u32 s50, s50, s40\n"
      "s_add_u32 s50, s50, s38\n"
      "s_add_u32 s50, s50, 32768\n"
      "s_add_u32 s51, s40, 98304\n"
      "s_lshr_b32 s52, s83, 1\n"
      "s_lshl_b32 s52, s52, 12\n"
      "s_add_u32 s52, s52, s38\n"
      "s_add_u32 s52, s52, 73728\n"
      "s_lshl_b32 s53, s84, 12\n"
      "s_lshl_b32 s40, s85, 7\n"
      "s_add_u32 s53, s53, s40\n"
      "s_add_u32 s53, s53, 65536\n"
      "s_mov_b32 s59, 65536\n"
      "s_waitcnt lgkmcnt(0)\n"
      "s_barrier\n"
      "s_cmp_eq_u32 s86, 2\n"
      "s_cbranch_scc1 .Lfk_F84\n"
      "s_cmp_eq_u32 s86, 1\n"
      "s_cbranch_scc0 .Lfk_fin86\n"
      ".Lfkpo1a:\n"
      "s_cbranch_execz .Lfkpa1d\n"
      "s_cmp_gt_u32 s39, 3\n"
      "s_cbranch_scc1 .Lfk_bo87\n"
      "s_memrealtime s[92:93]\n"
      "s_add_u32 s91, s90, 292\n"
      "s_waitcnt lgkmcnt(0)\n"
      "s_sub_u32 s92, s92, s91\n"
      "s_cmp_gt_i32 s92, 0\n"
      "s_cselect_b32 s93, 14, 0\n"
      "s_max_i32 s92, s92, 0\n"
      "s_add_u32 s90, s90, s92\n"
      "s_add_u32 s90, s90, s93\n"
      "s_add_u32 s91, s90, 316\n"
      ".Lfk_un88:\n"
      "s_memrealtime s[92:93]\n"
      "s_waitcnt lgkmcnt(0)\n"
      "s_sub_u32 s92, s92, s91\n"
      "s_cmp_gt_u32 s92, 4294965295\n"
      "s_cbranch_scc1 .Lfk_un88\n"
      ".Lfk_og89:\n"
      "s_mov_b32 m0, s51\n"
      "s_nop 0\n"
      "buffer_load_dwordx4 v1, s[24:27], s50 offen sc1 lds\n"
      "buffer_load_dwordx4 v1, s[24:27], s50 offen offset:1024 sc1 lds\n"
      "buffer_load_dwordx4 v[124:127], v114, s[24:27], s52 offen sc1\n"
      "s_waitcnt vmcnt(0)\n"
      "ds_read_b128 v[128:131], v115\n"
      "ds_read_b128 v[132:135], v116\n"
      "v_max_f32_dpp v136, v124, v124 quad_perm:[1,0,3,2] row_mask:0xf bank_mask:0xf\n"
      "v_xor_b32 v125, s23, v125\n"
      "v_xor_b32 v127, s23, v127\n"
      "v_max_f32_dpp v136, v136, v136 quad_perm:[2,3,0,1] row_mask:0xf bank_mask:0xf\n"
      "v_or_b32 v125, v125, v127\n"
      "v_mov_b32 v137, v136\n"
      "v_cmp_ne_u32 s[56:57], 0, v125\n"
      "s_nop 0\n"
      "v_permlane16_swap_b32 v136, v137\n"
      "v_max_f32 v136, v136, v137\n"
      "v_mov_b32 v137, v136\n"
      "s_waitcnt lgkmcnt(1)\n"
      "v_mfma_f32_16x16x32_bf16 v[148:151], v[128:131], v[4:7], 0\n"
      "v_max3_u32 v138, v128, v129, v130\n"
      "v_mfma_f32_16x16x32_bf16 v[152:155], v[128:131], v[8:11], 0\n"
      "v_permlane32_swap_b32 v136, v137\n"
      "v_max_f32 v136, v136, v137\n"
      "v_sub_f32 v137, v124, v136\n"
      "v_mfma_f32_16x16x32_bf16 v[156:159], v[128:131], v[12:15], 0\n"
      "v_exp_f32 v137, v137\n"
      "v_mfma_f32_16x16x32_bf16 v[160:163], v[128:131], v[16:19], 0\n"
      "v_max_u32 v138, v138, v131\n"
      "s_waitcnt lgkmcnt(0)\n"
      "v_mfma_f32_16x16x32_bf16 v[148:151], v[132:135], v[20:23], v[148:151]\n"
      "v_add_f32_dpp v139, v137, v137 quad_perm:[1,0,3,2] row_mask:0xf bank_mask:0xf\n"
      "v_max3_u32 v138, v138, v132, v133\n"
      "v_mfma_f32_16x16x32_bf16 v[152:155], v[132:135], v[24:27], v[152:155]\n"
      "v_add_f32_dpp v139, v139, v139 quad_perm:[2,3,0,1] row_mask:0xf bank_mask:0xf\n"
      "v_max3_u32 v138, v138, v134, v135\n"
      "v_mfma_f32_16x16x32_bf16 v[156:159], v[132:135], v[28:31], v[156:159]\n"
      "v_mov_b32 v140, v139\n"
      "v_cmp_eq_u32 vcc, -1, v138\n"
      "v_mfma_f32_16x16x32_bf16 v[160:163], v[132:135], v[32:35], v[160:163]\n"
      "v_permlane16_swap_b32 v139, v140\n"
      "s_or_b64 vcc, vcc, s[56:57]\n"
      "s_cbranch_vccnz .Lfk_ort91\n"
      ".Lfk_od90:\n"
      "v_add_f32 v139, v139, v140\n"
      "s_nop 1\n"
      "v_mov_b32 v140, v139\n"
      "v_mul_f32_dpp v128, v137, v148 quad_perm:[0,0,0,0] row_mask:0xf bank_mask:0xf\n"
      "v_mul_f32_dpp v130, v137, v156 quad_perm:[0,0,0,0] row_mask:0xf bank_mask:0xf\n"
      "v_mul_f32_dpp v129, v137, v152 quad_perm:[0,0,0,0] row_mask:0xf bank_mask:0xf\n"
      "v_mul_f32_dpp v131, v137, v160 quad_perm:[0,0,0,0] row_mask:0xf bank_mask:0xf\n"
      "v_permlane32_swap_b32 v139, v140\n"
      "v_add_f32 v139, v139, v140\n"
      "v_fmac_f32_dpp v128, v137, v149 quad_perm:[1,1,1,1] row_mask:0xf bank_mask:0xf\n"
      "v_fmac_f32_dpp v130, v137, v157 quad_perm:[1,1,1,1] row_mask:0xf bank_mask:0xf\n"
      "v_fmac_f32_dpp v129, v137, v153 quad_perm:[1,1,1,1] row_mask:0xf bank_mask:0xf\n"
      "v_fmac_f32_dpp v131, v137, v161 quad_perm:[1,1,1,1] row_mask:0xf bank_mask:0xf\n"
      "v_fmac_f32_dpp v128, v137, v150 quad_perm:[2,2,2,2] row_mask:0xf bank_mask:0xf\n"
      "v_fmac_f32_dpp v130, v137, v158 quad_perm:[2,2,2,2] row_mask:0xf bank_mask:0xf\n"
      "v_fmac_f32_dpp v129, v137, v154 quad_perm:[2,2,2,2] row_mask:0xf bank_mask:0xf\n"
      "v_fmac_f32_dpp v131, v137, v162 quad_perm:[2,2,2,2] row_mask:0xf bank_mask:0xf\n"
      "v_fmac_f32_dpp v128, v137, v151 quad_perm:[3,3,3,3] row_mask:0xf bank_mask:0xf\n"
      "v_fmac_f32_dpp v130, v137, v159 quad_perm:[3,3,3,3] row_mask:0xf bank_mask:0xf\n"
      "v_fmac_f32_dpp v129, v137, v155 quad_perm:[3,3,3,3] row_mask:0xf bank_mask:0xf\n"
      "v_fmac_f32_dpp v131, v137, v163 quad_perm:[3,3,3,3] row_mask:0xf bank_mask:0xf\n"
      "s_lshl_b32 s40, s88, 2\n"
      "s_add_u32 s40, s40, 1344\n"
      "v_mov_b32 v140, s40\n"
      "v_permlane32_swap_b32 v128, v130\n"
      "v_permlane32_swap_b32 v129, v131\n"
      "v_add_f32 v128, v128, v130\n"
      "v_add_f32 v129, v129, v131\n"
      "s_mov_b64 exec, 1\n"
      "ds_write2_b32 v140, v136, v139 offset0:0 offset1:2\n"
      "s_mov_b64 exec, -1\n"
      "v_permlane16_swap_b32 v128, v129\n"
      "v_add_f32 v128, v128, v129\n"
      "ds_write_b32 v108, v128 offset:12288\n"
      ".Lfk_bo87:\n"
      ".Lfkpo1c:\n"
      "s_cbranch_execz .Lfkpret\n"
      "s_waitcnt lgkmcnt(0)\n"
      "s_barrier\n"
      "s_cmp_eq_u32 s39, 0\n"
      "s_cbranch_scc0 .Lfk_oe92\n"
      "v_mov_b32 v120, 0\n"
      "ds_read_b128 v[124:127], v120 offset:1344\n"
      "ds_read2st64_b32 v[128:129], v109 offset0:48 offset1:49\n"
      "ds_read2st64_b32 v[130:131], v109 offset0:50 offset1:51\n"
      "s_waitcnt lgkmcnt(2)\n"
      "v_max_f32 v136, v124, v125\n"
      "v_sub_f32 v124, v124, v136\n"
      "v_sub_f32 v125, v125, v136\n"
      "v_exp_f32 v124, v124\n"
      "v_exp_f32 v125, v125\n"
      "s_waitcnt lgkmcnt(0)\n"
      "v_pk_add_f32 v[128:129], v[128:129], v[130:131]\n"
      "v_pk_mul_f32 v[126:127], v[126:127], v[124:125]\n"
      "v_pk_mul_f32 v[128:129], v[128:129], v[124:125]\n"
      "v_add_f32 v126, v126, v127\n"
      "v_rcp_f32 v126, v126\n"
      "v_add_f32 v128, v128, v129\n"
      "v_mul_f32 v128, v128, v126\n"
      "s_nop 1\n"
      "v_mov_b32_dpp v129, v128 quad_perm:[1,0,3,2] row_mask:0xf bank_mask:0xf\n"
      "v_cvt_pk_bf16_f32 v128, v128, v129\n"
      "v_min_u32 v128, -2, v128\n"
      "buffer_store_dword v128, v110, s[24:27], s53 offen sc1\n"
      ".Lfk_oe92:\n"
      "s_branch .Lfk_fin86\n"
      ".Lfk_rt58:\n"
      "s_sub_u32 s59, s59, 1\n"
      "s_cbranch_scc1 .Lfk_cs57\n"
      "s_sleep 2\n"
      "s_waitcnt lgkmcnt(0)\n"
      "s_mov_b64 exec, vcc\n"
      "buffer_load_dwordx4 v[124:127], v120, s[24:27], s50 offen sc1\n"
      "buffer_load_dwordx4 v[128:131], v120, s[24:27], s50 offen offset:256 sc1\n"
      "s_mov_b64 exec, -1\n"
      "s_branch .Lfk_cs56\n"
      ".Lfk_rt69:\n"
      "s_sub_u32 s59, s59, 1\n"
      "s_cbranch_scc1 .Lfk_cd68\n"
      "s_sleep 2\n"
      "s_waitcnt lgkmcnt(0)\n"
      "s_mov_b64 exec, vcc\n"
      "buffer_load_dwordx4 v[148:151], v1, s[24:27], s51 offen offset:0 sc1\n"
      "buffer_load_dwordx4 v[152:155], v1, s[24:27], s51 offen offset:1024 sc1\n"
      "buffer_load_dwordx4 v[156:159], v1, s[24:27], s51 offen offset:2048 sc1\n"
      "buffer_load_dwordx4 v[160:163], v113, s[24:27], s51 offen offset:3072 sc1\n"
      "s_mov_b64 exec, -1\n"
      "s_branch .Lfk_cg67\n"
      ".Lfk_rt73:\n"
      "s_sub_u32 s59, s59, 1\n"
      "s_cbranch_scc1 .Lfk_cd72\n"
      "s_sleep 2\n"
      "s_waitcnt lgkmcnt(0)\n"
      "s_mov_b64 exec, vcc\n"
      "buffer_load_dwordx4 v[148:151], v1, s[24:27], s51 offen offset:0 sc1\n"
      "buffer_load_dwordx4 v[152:155], v1, s[24:27], s51 offen offset:1024 sc1\n"
      "buffer_load_dwordx4 v[156:159], v1, s[24:27], s51 offen offset:2048 sc1\n"
      "buffer_load_dwordx4 v[160:163], v113, s[24:27], s51 offen offset:3072 sc1\n"
      "s_mov_b64 exec, -1\n"
      "s_branch .Lfk_cg71\n"
      ".Lfk_q376:\n"
      "s_add_u32 s40, s53, 0x1000\n"
      "v_mov_b32_dpp v156, v141 quad_perm:[1,0,3,2] row_mask:0xf bank_mask:0xf\n"
      "v_cvt_pk_bf16_f32 v144, v141, v156\n"
      "buffer_store_dwordx2 v[144:145], v111, s[24:27], s40 offen sc1\n"
      "s_waitcnt lgkmcnt(0)\n"
      "v_mov_b32 v126, 0\n"
      "v_mov_b32 v127, 0\n"
      "s_branch .Lfk_q377\n"
      ".Lfk_rt83:\n"
      "s_sub_u32 s59, s59, 1\n"
      "s_cbranch_scc1 .Lfk_qd82\n"
      "s_sleep 2\n"
      "s_waitcnt lgkmcnt(0)\n"
      "s_mov_b64 exec, vcc\n"
      "buffer_load_dwordx4 v[120:123], v1, s[24:27], s52 offen sc1\n"
      "s_mov_b64 exec, -1\n"
      "s_branch .Lfk_qg81\n"
      ".Lfk_ort91:\n"
      "s_sub_u32 s59, s59, 1\n"
      "s_cbranch_scc1 .Lfk_od90\n"
      "s_sleep 2\n"
      "s_branch .Lfk_og89\n"
      "s_branch .Lfkpf1a\n"
      ".p2align 12\n"
      ".Lfk_F84:\n"
      ".Lfkpf1a:\n"
      "s_cbranch_execz .Lfkpret\n"
      "s_lshl_b32 s40, s39, 12\n"
      "s_add_u32 s51, s40, 65536\n"
      "s_add_u32 s53, s40, 122880\n"
      "s_lshl_b32 s40, s82, 7\n"
      "s_add_u32 s52, s53, s40\n"
      "s_add_u32 s53, s53, 3584\n"
      "s_cmp_eq_u32 s39, 7\n"
      "s_cbranch_scc1 .Lfk_f793\n"
      "s_memrealtime s[92:93]\n"
      "s_add_u32 s91, s90, 299\n"
      "s_waitcnt lgkmcnt(0)\n"
      "s_sub_u32 s92, s92, s91\n"
      "s_cmp_gt_i32 s92, 0\n"
      "s_cselect_b32 s93, 14, 0\n"
      "s_max_i32 s92, s92, 0\n"
      "s_add_u32 s90, s90, s92\n"
      "s_add_u32 s90, s90, s93\n"
      "s_add_u32 s91, s90, 424\n"
      ".Lfk_un94:\n"
      "s_memrealtime s[92:93]\n"
      "s_waitcnt lgkmcnt(0)\n"
      "s_sub_u32 s92, s92, s91\n"
      "s_cmp_gt_u32 s92, 4294965295\n"
      "s_cbranch_scc1 .Lfk_un94\n"
      "buffer_load_dwordx4 v[148:151], v1, s[24:27], s51 offen offset:0 sc1\n"
      "buffer_load_dwordx4 v[152:155], v1, s[24:27], s51 offen offset:1024 sc1\n"
      "buffer_load_dwordx4 v[156:159], v1, s[24:27], s51 offen offset:2048 sc1\n"
      "buffer_load_dwordx4 v[160:163], v1, s[24:27], s51 offen offset:3072 sc1\n"
      "s_mov_b32 s59, 65536\n"
      ".Lfk_fg95:\n"
      "s_waitcnt vmcnt(3)\n"
      "v_lshlrev_b32 v128, 16, v148\n"
      "v_lshlrev_b32 v132, 16, v150\n"
      "v_and_b32 v129, 0xffff0000, v148\n"
      "v_and_b32 v133, 0xffff0000, v150\n"
      "v_lshlrev_b32 v130, 16, v149\n"
      "v_lshlrev_b32 v134, 16, v151\n"
      "v_and_b32 v131, 0xffff0000, v149\n"
      "v_and_b32 v135, 0xffff0000, v151\n"
      "s_waitcnt vmcnt(2)\n"
      "v_dot2c_f32_bf16 v128, s76, v152\n"
      "v_dot2c_f32_bf16 v132, s76, v154\n"
      "v_dot2c_f32_bf16 v129, s77, v152\n"
      "v_dot2c_f32_bf16 v133, s77, v154\n"
      "v_dot2c_f32_bf16 v130, s76, v153\n"
      "v_dot2c_f32_bf16 v134, s76, v155\n"
      "v_dot2c_f32_bf16 v131, s77, v153\n"
      "v_dot2c_f32_bf16 v135, s77, v155\n"
      "s_waitcnt vmcnt(1)\n"
      "v_dot2c_f32_bf16 v128, s76, v156\n"
      "v_dot2c_f32_bf16 v132, s76, v158\n"
      "v_dot2c_f32_bf16 v129, s77, v156\n"
      "v_dot2c_f32_bf16 v133, s77, v158\n"
      "v_dot2c_f32_bf16 v130, s76, v157\n"
      "v_dot2c_f32_bf16 v134, s76, v159\n"
      "v_dot2c_f32_bf16 v131, s77, v157\n"
      "v_dot2c_f32_bf16 v135, s77, v159\n"
      "s_waitcnt vmcnt(0)\n"
      "v_dot2c_f32_bf16 v128, s76, v160\n"
      "v_dot2c_f32_bf16 v132, s76, v162\n"
      "v_dot2c_f32_bf16 v129, s77, v160\n"
      "v_dot2c_f32_bf16 v133, s77, v162\n"
      "v_dot2c_f32_bf16 v130, s76, v161\n"
      "v_dot2c_f32_bf16 v134, s76, v163\n"
      "v_dot2c_f32_bf16 v131, s77, v161\n"
      "v_dot2c_f32_bf16 v135, s77, v163\n"
      "v_permlane32_swap_b32 v128, v132\n"
      "v_permlane32_swap_b32 v129, v133\n"
      "v_permlane32_swap_b32 v130, v134\n"
      "v_permlane32_swap_b32 v131, v135\n"
      "v_add_f32 v128, v128, v132\n"
      "v_add_f32 v130, v130, v134\n"
      "v_add_f32 v129, v129, v133\n"
      "v_add_f32 v131, v131, v135\n"
      "v_permlane16_swap_b32 v128, v130\n"
      "v_max3_u32 v138, v148, v149, v150\n"
      "v_permlane16_swap_b32 v129, v131\n"
      "v_add_f32 v128, v128, v130\n"
      "v_add_f32 v129, v129, v131\n"
      "v_cndmask_b32 v130, v129, v128, s[64:65]\n"
      "v_cndmask_b32 v131, v128, v129, s[64:65]\n"
      "v_add_f32 v131, v131, v117\n"
      "v_add_f32_dpp v140, v130, v131 row_ror:8 row_mask:0xf bank_mask:0xf\n"
      "v_cvt_pk_bf16_f32 v141, v140, v140\n"
      "ds_write_b16 v104, v141 offset:10240\n"
      "ds_read_b128 v[128:131], v105 offset:10240\n"
      "ds_read_b128 v[132:135], v105 offset:10304\n"
      "v_max3_u32 v139, v151, v152, v153\n"
      "v_max3_u32 v138, v138, v154, v155\n"
      "v_max3_u32 v139, v139, v156, v157\n"
      "v_max3_u32 v138, v138, v158, v159\n"
      "v_max3_u32 v139, v139, v160, v161\n"
      "v_max3_u32 v138, v138, v162, v163\n"
      "v_max_u32 v138, v138, v139\n"
      "v_cmp_eq_u32 vcc, -1, v138\n"
      "v_lshlrev_b32 v142, 16, v141\n"
      "s_cbranch_vccnz .Lfk_rt97\n"
      ".Lfk_fd96:\n"
      "v_mul_f32 v143, v142, v142\n"
      ".Lfkpf1d:\n"
      "s_cbranch_execz .Lfkpret\n"
      "s_waitcnt lgkmcnt(1)\n"
      "v_mfma_f32_16x16x32_bf16 v[148:151], v[128:131], v[4:7], 0\n"
      "v_add_f32_dpp v142, v142, v142 row_ror:8 row_mask:0xf bank_mask:0xf\n"
      "v_add_f32_dpp v143, v143, v143 row_ror:8 row_mask:0xf bank_mask:0xf\n"
      "v_mfma_f32_16x16x32_bf16 v[152:155], v[128:131], v[8:11], 0\n"
      "v_add_f32_dpp v142, v142, v142 row_ror:4 row_mask:0xf bank_mask:0xf\n"
      "v_add_f32_dpp v143, v143, v143 row_ror:4 row_mask:0xf bank_mask:0xf\n"
      "v_mfma_f32_16x16x32_bf16 v[156:159], v[128:131], v[12:15], 0\n"
      "v_add_f32_dpp v142, v142, v142 row_ror:2 row_mask:0xf bank_mask:0xf\n"
      "v_add_f32_dpp v143, v143, v143 row_ror:2 row_mask:0xf bank_mask:0xf\n"
      "v_mfma_f32_16x16x32_bf16 v[160:163], v[128:131], v[16:19], 0\n"
      "v_add_f32_dpp v142, v142, v142 row_ror:1 row_mask:0xf bank_mask:0xf\n"
      "v_add_f32_dpp v143, v143, v143 row_ror:1 row_mask:0xf bank_mask:0xf\n"
      "s_waitcnt lgkmcnt(0)\n"
      "v_mfma_f32_16x16x32_bf16 v[148:151], v[132:135], v[20:23], v[148:151]\n"
      "v_mov_b32 v144, v142\n"
      "v_mov_b32 v145, v143\n"
      "v_mfma_f32_16x16x32_bf16 v[152:155], v[132:135], v[24:27], v[152:155]\n"
      "v_permlane16_swap_b32 v142, v144\n"
      "v_permlane16_swap_b32 v143, v145\n"
      "v_add_f32 v142, v142, v144\n"
      "v_add_f32 v143, v143, v145\n"
      "v_mfma_f32_16x16x32_bf16 v[156:159], v[132:135], v[28:31], v[156:159]\n"
      "v_mov_b32 v144, v142\n"
      "v_mov_b32 v145, v143\n"
      "v_mfma_f32_16x16x32_bf16 v[160:163], v[132:135], v[32:35], v[160:163]\n"
      "v_permlane32_swap_b32 v142, v144\n"
      "v_permlane32_swap_b32 v143, v145\n"
      "v_add_f32 v142, v142, v144\n"
      "v_add_f32 v143, v143, v145\n"
      "s_lshl_b32 s40, s39, 3\n"
      "v_mov_b32 v144, s40\n"
      "s_mov_b64 exec, 1\n"
      "ds_write_b64 v144, v[142:143] offset:1344\n"
      "s_mov_b64 exec, -1\n"
      "s_cmp_eq_u32 s82, 0\n"
      "s_cbranch_scc0 .Lfk_nx98\n"
      "s_lshl_b32 s40, s39, 7\n"
      "v_and_b32 v120, 0x7c, v109\n"
      "v_add_u32 v120, s40, v120\n"
      "ds_read_b32 v120, v120 offset:10240\n"
      "s_waitcnt lgkmcnt(0)\n"
      "v_min_u32 v120, -2, v120\n"
      "buffer_store_dword v120, v112, s[24:27], s53 offen sc1\n"
      ".Lfk_nx98:\n"
      "v_cndmask_b32 v128, v148, v152, s[60:61]\n"
      "v_cndmask_b32 v129, v156, v160, s[60:61]\n"
      "v_cndmask_b32 v128, v128, v129, s[62:63]\n"
      "ds_write_b32 v108, v128 offset:12288\n"
      ".Lfkpf1e:\n"
      "s_cbranch_execz .Lfkpret\n"
      "s_waitcnt lgkmcnt(0)\n"
      "s_barrier\n"
      "v_mov_b32 v120, 0\n"
      "ds_read_b128 v[124:127], v120 offset:1344\n"
      "ds_read_b128 v[128:131], v120 offset:1360\n"
      "ds_read_b128 v[132:135], v120 offset:1376\n"
      "ds_read_b64 v[136:137], v120 offset:1392\n"
      "ds_read2st64_b32 v[138:139], v109 offset0:48 offset1:49\n"
      "ds_read2st64_b32 v[140:141], v109 offset0:50 offset1:51\n"
      "ds_read2st64_b32 v[142:143], v109 offset0:52 offset1:53\n"
      "ds_read_b32 v144, v109 offset:13824\n"
      "s_waitcnt lgkmcnt(4)\n"
      "v_pk_add_f32 v[124:125], v[124:125], v[126:127]\n"
      "v_pk_add_f32 v[128:129], v[128:129], v[130:131]\n"
      "v_pk_add_f32 v[132:133], v[132:133], v[134:135]\n"
      "v_pk_add_f32 v[124:125], v[124:125], v[128:129]\n"
      "v_pk_add_f32 v[132:133], v[132:133], v[136:137]\n"
      "v_pk_add_f32 v[124:125], v[124:125], v[132:133]\n"
      "v_mul_f32 v124, s78, v124\n"
      "s_waitcnt lgkmcnt(0)\n"
      "v_pk_add_f32 v[138:139], v[138:139], v[140:141]\n"
      "v_mul_f32 v125, s78, v125\n"
      "v_fma_f32 v125, -v124, v124, v125\n"
      "v_max_f32 v125, 0, v125\n"
      "v_add_f32 v125, s79, v125\n"
      "v_rsq_f32 v125, v125\n"
      "v_pk_add_f32 v[138:139], v[138:139], v[142:143]\n"
      "v_add_f32 v138, v138, v144\n"
      "v_add_f32 v138, v138, v139\n"
      "v_fma_f32 v138, -v124, v118, v138\n"
      "v_mul_f32 v138, v138, v125\n"
      "v_mul_f32 v139, s80, v138\n"
      "v_exp_f32 v139, v139\n"
      "s_nop 0\n"
      "v_add_f32 v139, 1.0, v139\n"
      "v_rcp_f32 v139, v139\n"
      "s_nop 0\n"
      "v_mul_f32 v138, v138, v139\n"
      "v_cvt_pk_bf16_f32 v138, v138, v138\n"
      "ds_write_b16 v106, v138 offset:11264\n"
      "ds_read_b128 v[128:131], v105 offset:11264\n"
      "ds_read_b128 v[132:135], v105 offset:11328\n"
      "s_waitcnt lgkmcnt(1)\n"
      "v_mfma_f32_16x16x32_bf16 v[148:151], v[128:131], v[36:39], 0\n"
      "v_mfma_f32_16x16x32_bf16 v[152:155], v[128:131], v[40:43], 0\n"
      "v_mfma_f32_16x16x32_bf16 v[156:159], v[128:131], v[44:47], 0\n"
      "v_mfma_f32_16x16x32_bf16 v[160:163], v[128:131], v[48:51], 0\n"
      "s_waitcnt lgkmcnt(0)\n"
      "v_mfma_f32_16x16x32_bf16 v[148:151], v[132:135], v[52:55], v[148:151]\n"
      "v_mfma_f32_16x16x32_bf16 v[152:155], v[132:135], v[56:59], v[152:155]\n"
      "v_mfma_f32_16x16x32_bf16 v[156:159], v[132:135], v[60:63], v[156:159]\n"
      "v_mfma_f32_16x16x32_bf16 v[160:163], v[132:135], v[64:67], v[160:163]\n"
      "s_nop 5\n"
      "v_cndmask_b32 v128, v148, v152, s[60:61]\n"
      "s_nop 0\n"
      "v_cndmask_b32 v129, v156, v160, s[60:61]\n"
      "v_cndmask_b32 v128, v128, v129, s[62:63]\n"
      "s_nop 1\n"
      "v_mov_b32_dpp v129, v128 quad_perm:[1,0,3,2] row_mask:0xf bank_mask:0xf\n"
      "v_cvt_pk_bf16_f32 v128, v128, v129\n"
      "v_min_u32 v128, -2, v128\n"
      "buffer_store_dword v128, v110, s[24:27], s52 offen sc1\n"
      "s_branch .Lfk_E85\n"
      ".Lfk_f793:\n"
      "s_waitcnt lgkmcnt(0)\n"
      "s_barrier\n"
      "s_branch .Lfk_E85\n"
      ".Lfk_rt97:\n"
      "s_sub_u32 s59, s59, 1\n"
      "s_cbranch_scc1 .Lfk_fd96\n"
      "s_sleep 2\n"
      "s_waitcnt lgkmcnt(0)\n"
      "s_mov_b64 exec, vcc\n"
      "buffer_load_dwordx4 v[148:151], v1, s[24:27], s51 offen offset:0 sc1\n"
      "buffer_load_dwordx4 v[152:155], v1, s[24:27], s51 offen offset:1024 sc1\n"
      "buffer_load_dwordx4 v[156:159], v1, s[24:27], s51 offen offset:2048 sc1\n"
      "buffer_load_dwordx4 v[160:163], v1, s[24:27], s51 offen offset:3072 sc1\n"
      "s_waitcnt vmcnt(0)\n"
      "s_mov_b64 exec, -1\n"
      "s_branch .Lfk_fg95\n"
      ".Lfk_fin86:\n"
      "s_cmp_eq_u32 s37, 20\n"
      "s_cbranch_scc0 .Lfk_E85\n"
      "s_cmp_eq_u32 s39, s36\n"
      "s_cbranch_scc0 .Lfk_E85\n"
      "s_cmp_eq_u32 s39, 7\n"
      "s_cbranch_scc1 .Lfk_E85\n"
      ".Lfkpz0:\n"
      "s_cbranch_execz .Lfkpret\n"
      "s_lshl_b32 s40, s39, 12\n"
      "s_add_u32 s51, s40, 122880\n"
      "s_memrealtime s[92:93]\n"
      "s_add_u32 s91, s90, 386\n"
      "s_waitcnt lgkmcnt(0)\n"
      "s_sub_u32 s92, s92, s91\n"
      "s_cmp_gt_i32 s92, 0\n"
      "s_cselect_b32 s93, 14, 0\n"
      "s_max_i32 s92, s92, 0\n"
      "s_add_u32 s90, s90, s92\n"
      "s_add_u32 s90, s90, s93\n"
      "s_add_u32 s91, s90, 571\n"
      ".Lfk_un99:\n"
      "s_memrealtime s[92:93]\n"
      "s_waitcnt lgkmcnt(0)\n"
      "s_sub_u32 s92, s92, s91\n"
      "s_cmp_gt_u32 s92, 4294965295\n"
      "s_cbranch_scc1 .Lfk_un99\n"
      "buffer_load_dwordx4 v[148:151], v1, s[24:27], s51 offen offset:0 sc1\n"
      "buffer_load_dwordx4 v[152:155], v1, s[24:27], s51 offen offset:1024 sc1\n"
      "buffer_load_dwordx4 v[156:159], v1, s[24:27], s51 offen offset:2048 sc1\n"
      "buffer_load_dwordx4 v[160:163], v113, s[24:27], s51 offen offset:3072 sc1\n"
      "s_mov_b32 s59, 65536\n"
      ".Lfk_cg100:\n"
      "s_waitcnt vmcnt(3)\n"
      "v_lshlrev_b32 v128, 16, v148\n"
      "v_lshlrev_b32 v132, 16, v150\n"
      "v_and_b32 v129, 0xffff0000, v148\n"
      "v_and_b32 v133, 0xffff0000, v150\n"
      "v_lshlrev_b32 v130, 16, v149\n"
      "v_lshlrev_b32 v134, 16, v151\n"
      "v_and_b32 v131, 0xffff0000, v149\n"
      "v_and_b32 v135, 0xffff0000, v151\n"
      "s_waitcnt vmcnt(2)\n"
      "v_dot2c_f32_bf16 v128, s76, v152\n"
      "v_dot2c_f32_bf16 v132, s76, v154\n"
      "v_dot2c_f32_bf16 v129, s77, v152\n"
      "v_dot2c_f32_bf16 v133, s77, v154\n"
      "v_dot2c_f32_bf16 v130, s76, v153\n"
      "v_dot2c_f32_bf16 v134, s76, v155\n"
      "v_dot2c_f32_bf16 v131, s77, v153\n"
      "v_dot2c_f32_bf16 v135, s77, v155\n"
      "s_waitcnt vmcnt(1)\n"
      "v_dot2c_f32_bf16 v128, s76, v156\n"
      "v_dot2c_f32_bf16 v132, s76, v158\n"
      "v_dot2c_f32_bf16 v129, s77, v156\n"
      "v_dot2c_f32_bf16 v133, s77, v158\n"
      "v_dot2c_f32_bf16 v130, s76, v157\n"
      "v_dot2c_f32_bf16 v134, s76, v159\n"
      "v_dot2c_f32_bf16 v131, s77, v157\n"
      "v_dot2c_f32_bf16 v135, s77, v159\n"
      "s_waitcnt vmcnt(0)\n"
      "v_dot2c_f32_bf16 v128, s76, v160\n"
      "v_dot2c_f32_bf16 v132, s76, v162\n"
      "v_dot2c_f32_bf16 v129, s77, v160\n"
      "v_dot2c_f32_bf16 v133, s77, v162\n"
      "v_dot2c_f32_bf16 v130, s76, v161\n"
      "v_dot2c_f32_bf16 v134, s76, v163\n"
      "v_dot2c_f32_bf16 v131, s77, v161\n"
      "v_dot2c_f32_bf16 v135, s77, v163\n"
      "v_permlane32_swap_b32 v128, v132\n"
      "v_permlane32_swap_b32 v129, v133\n"
      "v_permlane32_swap_b32 v130, v134\n"
      "v_permlane32_swap_b32 v131, v135\n"
      "v_add_f32 v128, v128, v132\n"
      "v_add_f32 v130, v130, v134\n"
      "v_add_f32 v129, v129, v133\n"
      "v_add_f32 v131, v131, v135\n"
      "v_permlane16_swap_b32 v128, v130\n"
      "v_max3_u32 v138, v148, v149, v150\n"
      "v_permlane16_swap_b32 v129, v131\n"
      "v_add_f32 v128, v128, v130\n"
      "v_add_f32 v129, v129, v131\n"
      "v_cndmask_b32 v130, v129, v128, s[64:65]\n"
      "v_cndmask_b32 v131, v128, v129, s[64:65]\n"
      "v_max3_u32 v139, v151, v152, v153\n"
      "v_add_f32_dpp v140, v130, v131 row_ror:8 row_mask:0xf bank_mask:0xf\n"
      "v_max3_u32 v138, v138, v154, v155\n"
      "v_max3_u32 v139, v139, v156, v157\n"
      "v_max3_u32 v138, v138, v158, v159\n"
      "v_max3_u32 v139, v139, v160, v161\n"
      "v_max3_u32 v138, v138, v162, v163\n"
      "v_max_u32 v138, v138, v139\n"
      "v_cmp_eq_u32 vcc, -1, v138\n"
      "v_cvt_pk_bf16_f32 v141, v140, v140\n"
      "s_cbranch_vccnz .Lfk_rt102\n"
      ".Lfk_cd101:\n"
      "buffer_store_short v141, v104, s[32:35], 0 offen\n"
      ".Lfk_E85:\n"
      "s_endpgm\n"
      ".Lfk_rt102:\n"
      "s_sub_u32 s59, s59, 1\n"
      "s_cbranch_scc1 .Lfk_cd101\n"
      "s_sleep 2\n"
      "s_waitcnt lgkmcnt(0)\n"
      "s_mov_b64 exec, vcc\n"
      "buffer_load_dwordx4 v[148:151], v1, s[24:27], s51 offen offset:0 sc1\n"
      "buffer_load_dwordx4 v[152:155], v1, s[24:27], s51 offen offset:1024 sc1\n"
      "buffer_load_dwordx4 v[156:159], v1, s[24:27], s51 offen offset:2048 sc1\n"
      "buffer_load_dwordx4 v[160:163], v113, s[24:27], s51 offen offset:3072 sc1\n"
      "s_mov_b64 exec, -1\n"
      "s_branch .Lfk_cg100\n"
      "s_branch .Lfkpa0\n"
      ".p2align 12\n"
      ".Lfk_att3:\n"
      ".Lfkpa0:\n"
      "s_cbranch_execz .Lfkpp3\n"
      "s_waitcnt lgkmcnt(0)\n"
      "s_mul_i32 s23, s23, 0x9e3779b5\n"
      "s_add_u32 s23, s23, 0x7f4a7c15\n"
      "v_and_b32 v132, 15, v0\n"
      "v_lshrrev_b32 v133, 4, v0\n"
      "v_xor_b32 v134, v133, v132\n"
      "v_lshlrev_b32 v134, 4, v134\n"
      "v_lshl_or_b32 v134, v132, 9, v134\n"
      "s_add_u32 s40, s42, 32768\n"
      "v_add_u32 v134, s40, v134\n"
      "v_xor_b32 v135, 64, v134\n"
      "v_xor_b32 v136, 0x80, v134\n"
      "v_xor_b32 v137, 0xc0, v134\n"
      "v_add_u32 v138, s48, v1\n"
      "s_cmp_eq_u32 s87, 4096\n"
      "s_cbranch_scc0 .Lfk_gw103\n"
      "s_waitcnt vmcnt(12)\n"
      ".Lfk_gw103:\n"
      "s_cmp_eq_u32 s87, 4096\n"
      "s_cbranch_scc0 .Lfk_gw104\n"
      "s_waitcnt vmcnt(12)\n"
      ".Lfk_gw104:\n"
      "s_cmp_eq_u32 s87, 11264\n"
      "s_cbranch_scc0 .Lfk_gw105\n"
      "s_waitcnt vmcnt(5)\n"
      ".Lfk_gw105:\n"
      "ds_read_b128 v[140:143], v138\n"
      "ds_read_b128 v[144:147], v138 offset:1024\n"
      "ds_read_b128 v[4:7], v134 offset:0\n"
      "ds_read_b128 v[8:11], v135 offset:0\n"
      "ds_read_b128 v[12:15], v136 offset:0\n"
      "ds_read_b128 v[16:19], v137 offset:0\n"
      "ds_read_b128 v[20:23], v134 offset:256\n"
      "ds_read_b128 v[24:27], v135 offset:256\n"
      "ds_read_b128 v[28:31], v136 offset:256\n"
      "ds_read_b128 v[32:35], v137 offset:256\n"
      "ds_read_b128 v[36:39], v134 offset:8192\n"
      "ds_read_b128 v[40:43], v135 offset:8192\n"
      "ds_read_b128 v[44:47], v136 offset:8192\n"
      "ds_read_b128 v[48:51], v137 offset:8192\n"
      "s_lshr_b32 s43, s22, 1\n"
      "s_and_b32 s44, s22, 1\n"
      "s_mul_i32 s40, s43, 576\n"
      "s_lshl_b32 s41, s44, 8\n"
      "s_add_u32 s40, s40, s41\n"
      "v_lshlrev_b32 v139, 3, v132\n"
      "v_lshl_or_b32 v139, v133, 1, v139\n"
      "v_add_u32 v139, s40, v139\n"
      "s_mov_b32 s59, 65536\n"
      "s_waitcnt lgkmcnt(12)\n"
      "ds_read_b128 v[52:55], v134 offset:8448\n"
      "ds_read_b128 v[56:59], v135 offset:8448\n"
      "ds_read_b128 v[60:63], v136 offset:8448\n"
      "ds_read_b128 v[64:67], v137 offset:8448\n"
      ".Lfk_q0106:\n"
      "v_lshlrev_b32 v148, 16, v140\n"
      "v_lshlrev_b32 v149, 16, v142\n"
      "v_and_b32 v150, 0xffff0000, v140\n"
      "v_and_b32 v151, 0xffff0000, v142\n"
      "v_cmp_ne_u32 vcc, s23, v141\n"
      "v_cmp_ne_u32 s[56:57], s23, v143\n"
      "v_permlane32_swap_b32 v148, v149\n"
      "v_permlane32_swap_b32 v150, v151\n"
      "v_add_f32 v148, v148, v149\n"
      "v_add_f32 v150, v150, v151\n"
      "s_nop 1\n"
      "v_permlane16_swap_b32 v148, v150\n"
      "v_add_f32 v148, v148, v150\n"
      "v_cvt_pk_bf16_f32 v148, v148, v148\n"
      "ds_write_b16 v139, v148 offset:0\n"
      "s_or_b64 s[54:55], vcc, s[56:57]\n"
      "v_lshlrev_b32 v152, 16, v144\n"
      "v_lshlrev_b32 v153, 16, v146\n"
      "v_and_b32 v154, 0xffff0000, v144\n"
      "v_and_b32 v155, 0xffff0000, v146\n"
      "v_cmp_ne_u32 vcc, s23, v145\n"
      "v_cmp_ne_u32 s[56:57], s23, v147\n"
      "v_permlane32_swap_b32 v152, v153\n"
      "v_permlane32_swap_b32 v154, v155\n"
      "v_add_f32 v152, v152, v153\n"
      "v_add_f32 v154, v154, v155\n"
      "s_nop 1\n"
      "v_permlane16_swap_b32 v152, v154\n"
      "v_add_f32 v152, v152, v154\n"
      "v_cvt_pk_bf16_f32 v152, v152, v152\n"
      "ds_write_b16 v139, v152 offset:128\n"
      "s_or_b64 vcc, vcc, s[56:57]\n"
      "s_or_b64 vcc, vcc, s[54:55]\n"
      "s_cbranch_vccnz .Lfk_rt108\n"
      ".Lfk_q0107:\n"
      "v_and_b32 v138, 1, v0\n"
      "v_mul_u32_u24 v138, 576, v138\n"
      "v_and_b32 v139, 48, v0\n"
      "v_add_u32 v1, v138, v139\n"
      "v_lshl_add_u32 v2, v0, 2, s48\n"
      "v_bfe_u32 v138, v0, 3, 1\n"
      "v_lshlrev_b32 v138, 8, v138\n"
      "v_lshl_or_b32 v138, v133, 6, v138\n"
      "v_and_b32 v139, 7, v0\n"
      "v_lshl_or_b32 v138, v139, 3, v138\n"
      "s_lshl_b32 s40, s22, 9\n"
      "s_add_u32 s40, s40, 2048\n"
      "v_add_u32 v3, s40, v138\n"
      "v_lshrrev_b32 v139, 5, v0\n"
      "v_lshlrev_b32 v139, 12, v139\n"
      "v_bfe_u32 v148, v0, 2, 3\n"
      "s_bfe_u32 s40, s37, 0x30001\n"
      "v_xor_b32 v148, s40, v148\n"
      "v_lshl_or_b32 v139, v148, 4, v139\n"
      "v_and_b32 v148, 3, v0\n"
      "v_lshl_or_b32 v139, v148, 2, v139\n"
      "s_lshl_b32 s40, s22, 13\n"
      "s_lshl_b32 s41, s37, 7\n"
      "s_add_u32 s40, s40, s41\n"
      "v_add_u32 v164, s40, v139\n"
      "s_lshl_b32 s45, s43, 12\n"
      "s_add_u32 s45, s45, s41\n"
      "s_add_u32 s45, s45, s38\n"
      "s_add_u32 s45, s45, 65536\n"
      "s_lshl_b32 s46, s22, 2\n"
      "s_add_u32 s46, s46, 1280\n"
      "s_lshl_b32 s49, s43, 5\n"
      "s_add_u32 s49, s49, 1280\n"
      "s_mov_b32 s81, 0x3db8aa3b\n"
      "s_mov_b32 s60, 0x30003\n"
      "s_mov_b32 s61, 0x30003\n"
      "s_mov_b32 s62, 0xc000c\n"
      "s_mov_b32 s63, 0xc000c\n"
      "s_mov_b32 s64, 0x300030\n"
      "s_mov_b32 s65, 0x300030\n"
      "s_mov_b32 s66, 0xc000c0\n"
      "s_mov_b32 s67, 0xc000c0\n"
      "s_mov_b32 s68, 0x3000300\n"
      "s_mov_b32 s69, 0x3000300\n"
      "s_mov_b32 s70, 0xc000c00\n"
      "s_mov_b32 s71, 0xc000c00\n"
      "s_mov_b32 s72, 0x30003000\n"
      "s_mov_b32 s73, 0x30003000\n"
      "s_mov_b32 s74, 0xc000c000\n"
      "s_mov_b32 s75, 0xc000c000\n"
      "s_add_u32 s50, s38, 0\n"
      "s_add_u32 s51, s45, 0\n"
      ".Lfkpa0b:\n"
      "s_cbranch_execz .Lfkpa0e\n"
      "s_waitcnt lgkmcnt(0)\n"
      "s_barrier\n"
      "ds_read_b128 v[132:135], v1 offset:0\n"
      "ds_read_b128 v[136:139], v1 offset:64\n"
      "ds_read_b128 v[140:143], v1 offset:128\n"
      "ds_read_b128 v[144:147], v1 offset:192\n"
      "s_waitcnt lgkmcnt(3)\n"
      "v_mfma_f32_16x16x32_bf16 v[148:151], v[4:7], v[132:135], 0\n"
      "v_mfma_f32_16x16x32_bf16 v[152:155], v[36:39], v[132:135], 0\n"
      "ds_read_b128 v[132:135], v1 offset:256\n"
      "s_waitcnt lgkmcnt(3)\n"
      "v_mfma_f32_16x16x32_bf16 v[148:151], v[8:11], v[136:139], v[148:151]\n"
      "v_mfma_f32_16x16x32_bf16 v[152:155], v[40:43], v[136:139], v[152:155]\n"
      "ds_read_b128 v[136:139], v1 offset:320\n"
      "s_waitcnt lgkmcnt(3)\n"
      "v_mfma_f32_16x16x32_bf16 v[148:151], v[12:15], v[140:143], v[148:151]\n"
      "v_mfma_f32_16x16x32_bf16 v[152:155], v[44:47], v[140:143], v[152:155]\n"
      "ds_read_b128 v[140:143], v1 offset:384\n"
      "s_waitcnt lgkmcnt(3)\n"
      "v_mfma_f32_16x16x32_bf16 v[148:151], v[16:19], v[144:147], v[148:151]\n"
      "v_mfma_f32_16x16x32_bf16 v[152:155], v[48:51], v[144:147], v[152:155]\n"
      "ds_read_b128 v[144:147], v1 offset:448\n"
      "s_waitcnt lgkmcnt(3)\n"
      "v_mfma_f32_16x16x32_bf16 v[148:151], v[20:23], v[132:135], v[148:151]\n"
      "v_mfma_f32_16x16x32_bf16 v[152:155], v[52:55], v[132:135], v[152:155]\n"
      "s_waitcnt lgkmcnt(2)\n"
      "v_mfma_f32_16x16x32_bf16 v[148:151], v[24:27], v[136:139], v[148:151]\n"
      "v_mfma_f32_16x16x32_bf16 v[152:155], v[56:59], v[136:139], v[152:155]\n"
      "s_waitcnt lgkmcnt(1)\n"
      "v_mfma_f32_16x16x32_bf16 v[148:151], v[28:31], v[140:143], v[148:151]\n"
      "v_mfma_f32_16x16x32_bf16 v[152:155], v[60:63], v[140:143], v[152:155]\n"
      "s_waitcnt lgkmcnt(0)\n"
      "v_mfma_f32_16x16x32_bf16 v[148:151], v[32:35], v[144:147], v[148:151]\n"
      "v_mfma_f32_16x16x32_bf16 v[152:155], v[64:67], v[144:147], v[152:155]\n"
      "s_nop 6\n"
      "v_max3_f32 v132, v148, v149, v150\n"
      "v_max3_f32 v133, v151, v152, v153\n"
      "v_max3_f32 v132, v132, v154, v155\n"
      "v_max_f32 v132, v132, v133\n"
      "v_mov_b32 v133, v132\n"
      "s_nop 1\n"
      "v_permlane16_swap_b32 v132, v133\n"
      "v_max_f32 v132, v132, v133\n"
      "v_mov_b32 v133, v132\n"
      "s_nop 1\n"
      "v_permlane32_swap_b32 v132, v133\n"
      "v_max_f32 v132, v132, v133\n"
      "v_mul_f32 v165, s81, v132\n"
      "v_fma_f32 v148, v148, s81, -v165\n"
      "v_fma_f32 v149, v149, s81, -v165\n"
      "v_exp_f32 v148, v148\n"
      "v_fma_f32 v150, v150, s81, -v165\n"
      "v_exp_f32 v149, v149\n"
      "v_fma_f32 v151, v151, s81, -v165\n"
      "v_exp_f32 v150, v150\n"
      "v_cvt_pk_bf16_f32 v156, v148, v149\n"
      "v_fma_f32 v152, v152, s81, -v165\n"
      "v_exp_f32 v151, v151\n"
      "v_fma_f32 v153, v153, s81, -v165\n"
      "v_exp_f32 v152, v152\n"
      "v_cvt_pk_bf16_f32 v157, v150, v151\n"
      "v_fma_f32 v154, v154, s81, -v165\n"
      "v_exp_f32 v153, v153\n"
      "v_fma_f32 v155, v155, s81, -v165\n"
      "v_exp_f32 v154, v154\n"
      "v_cvt_pk_bf16_f32 v158, v152, v153\n"
      "v_exp_f32 v155, v155\n"
      "v_cndmask_b32 v132, 0, v156, s[60:61]\n"
      "v_cndmask_b32 v133, 0, v157, s[60:61]\n"
      "v_cndmask_b32 v134, 0, v158, s[60:61]\n"
      "v_cvt_pk_bf16_f32 v159, v154, v155\n"
      "v_cndmask_b32 v136, 0, v156, s[62:63]\n"
      "v_cndmask_b32 v137, 0, v157, s[62:63]\n"
      "v_cndmask_b32 v138, 0, v158, s[62:63]\n"
      "v_cndmask_b32 v135, 0, v159, s[60:61]\n"
      "v_cndmask_b32 v139, 0, v159, s[62:63]\n"
      "v_lshrrev_b32 v140, 4, v0\n"
      "v_bfe_u32 v141, v0, 2, 2\n"
      "v_lshl_add_u32 v142, v140, 2, v141\n"
      "v_lshlrev_b32 v143, 9, v142\n"
      "v_and_b32 v142, 7, v142\n"
      "v_lshl_or_b32 v143, v142, 5, v143\n"
      "v_and_b32 v142, 3, v0\n"
      "v_lshl_or_b32 v143, v142, 3, v143\n"
      "s_add_u32 s40, s42, 98304\n"
      "v_add_u32 v144, s40, v143\n"
      "v_xor_b32 v145, 0x20, v144\n"
      "v_xor_b32 v146, 0x40, v144\n"
      "v_xor_b32 v147, 0x60, v144\n"
      "v_xor_b32 v160, 0x80, v144\n"
      "v_xor_b32 v161, 0xa0, v144\n"
      "v_xor_b32 v162, 0xc0, v144\n"
      "v_xor_b32 v163, 0xe0, v144\n"
      ".Lfkpa0c:\n"
      "s_cbranch_execz .Lfkpo0a\n"
      "s_waitcnt vmcnt(8)\n"
      "ds_read_b64_tr_b16 v[68:69], v144 offset:0\n"
      "ds_read_b64_tr_b16 v[72:73], v145 offset:0\n"
      "ds_read_b64_tr_b16 v[76:77], v146 offset:0\n"
      "ds_read_b64_tr_b16 v[80:81], v147 offset:0\n"
      "ds_read_b64_tr_b16 v[84:85], v160 offset:0\n"
      "ds_read_b64_tr_b16 v[88:89], v161 offset:0\n"
      "ds_read_b64_tr_b16 v[92:93], v162 offset:0\n"
      "ds_read_b64_tr_b16 v[96:97], v163 offset:0\n"
      "ds_read_b64_tr_b16 v[100:101], v144 offset:256\n"
      "ds_read_b64_tr_b16 v[104:105], v145 offset:256\n"
      "ds_read_b64_tr_b16 v[108:109], v146 offset:256\n"
      "ds_read_b64_tr_b16 v[112:113], v147 offset:256\n"
      "ds_read_b64_tr_b16 v[116:117], v160 offset:256\n"
      "ds_read_b64_tr_b16 v[120:121], v161 offset:256\n"
      "ds_read_b64_tr_b16 v[124:125], v162 offset:256\n"
      "ds_read_b64_tr_b16 v[128:129], v163 offset:256\n"
      "s_waitcnt vmcnt(0)\n"
      "ds_read_b64_tr_b16 v[70:71], v144 offset:8192\n"
      "ds_read_b64_tr_b16 v[102:103], v144 offset:8448\n"
      "ds_read_b64_tr_b16 v[74:75], v145 offset:8192\n"
      "ds_read_b64_tr_b16 v[106:107], v145 offset:8448\n"
      "ds_read_b64_tr_b16 v[78:79], v146 offset:8192\n"
      "ds_read_b64_tr_b16 v[110:111], v146 offset:8448\n"
      "ds_read_b64_tr_b16 v[82:83], v147 offset:8192\n"
      "ds_read_b64_tr_b16 v[114:115], v147 offset:8448\n"
      "ds_read_b64_tr_b16 v[86:87], v160 offset:8192\n"
      "ds_read_b64_tr_b16 v[118:119], v160 offset:8448\n"
      "ds_read_b64_tr_b16 v[90:91], v161 offset:8192\n"
      "ds_read_b64_tr_b16 v[122:123], v161 offset:8448\n"
      "ds_read_b64_tr_b16 v[94:95], v162 offset:8192\n"
      "ds_read_b64_tr_b16 v[126:127], v162 offset:8448\n"
      "ds_read_b64_tr_b16 v[98:99], v163 offset:8192\n"
      "ds_read_b64_tr_b16 v[130:131], v163 offset:8448\n"
      "s_waitcnt lgkmcnt(14)\n"
      "v_mfma_f32_16x16x32_bf16 v[144:147], v[132:135], v[68:71], 0\n"
      "v_cndmask_b32 v140, 0, v156, s[64:65]\n"
      "v_cndmask_b32 v141, 0, v157, s[64:65]\n"
      "v_mfma_f32_16x16x32_bf16 v[160:163], v[132:135], v[100:103], 0\n"
      "v_cndmask_b32 v142, 0, v158, s[64:65]\n"
      "v_cndmask_b32 v143, 0, v159, s[64:65]\n"
      "s_waitcnt lgkmcnt(12)\n"
      "v_mfma_f32_16x16x32_bf16 v[144:147], v[136:139], v[72:75], v[144:147]\n"
      "v_cndmask_b32 v132, 0, v156, s[66:67]\n"
      "v_cndmask_b32 v133, 0, v157, s[66:67]\n"
      "v_mfma_f32_16x16x32_bf16 v[160:163], v[136:139], v[104:107], v[160:163]\n"
      "v_cndmask_b32 v134, 0, v158, s[66:67]\n"
      "v_cndmask_b32 v135, 0, v159, s[66:67]\n"
      "s_waitcnt lgkmcnt(10)\n"
      "v_mfma_f32_16x16x32_bf16 v[144:147], v[140:143], v[76:79], v[144:147]\n"
      "v_cndmask_b32 v136, 0, v156, s[68:69]\n"
      "v_cndmask_b32 v137, 0, v157, s[68:69]\n"
      "v_mfma_f32_16x16x32_bf16 v[160:163], v[140:143], v[108:111], v[160:163]\n"
      "v_cndmask_b32 v138, 0, v158, s[68:69]\n"
      "v_cndmask_b32 v139, 0, v159, s[68:69]\n"
      "s_waitcnt lgkmcnt(8)\n"
      "v_mfma_f32_16x16x32_bf16 v[144:147], v[132:135], v[80:83], v[144:147]\n"
      "v_cndmask_b32 v140, 0, v156, s[70:71]\n"
      "v_cndmask_b32 v141, 0, v157, s[70:71]\n"
      "v_mfma_f32_16x16x32_bf16 v[160:163], v[132:135], v[112:115], v[160:163]\n"
      "v_cndmask_b32 v142, 0, v158, s[70:71]\n"
      "v_cndmask_b32 v143, 0, v159, s[70:71]\n"
      ".Lfkpa0d:\n"
      "s_cbranch_execz .Lfkpo0c\n"
      "s_waitcnt lgkmcnt(6)\n"
      "v_mfma_f32_16x16x32_bf16 v[144:147], v[136:139], v[84:87], v[144:147]\n"
      "v_cndmask_b32 v132, 0, v156, s[72:73]\n"
      "v_cndmask_b32 v133, 0, v157, s[72:73]\n"
      "v_mfma_f32_16x16x32_bf16 v[160:163], v[136:139], v[116:119], v[160:163]\n"
      "v_cndmask_b32 v134, 0, v158, s[72:73]\n"
      "v_cndmask_b32 v135, 0, v159, s[72:73]\n"
      "s_waitcnt lgkmcnt(4)\n"
      "v_mfma_f32_16x16x32_bf16 v[144:147], v[140:143], v[88:91], v[144:147]\n"
      "v_cndmask_b32 v136, 0, v156, s[74:75]\n"
      "v_cndmask_b32 v137, 0, v157, s[74:75]\n"
      "v_mfma_f32_16x16x32_bf16 v[160:163], v[140:143], v[120:123], v[160:163]\n"
      "v_cndmask_b32 v138, 0, v158, s[74:75]\n"
      "v_cndmask_b32 v139, 0, v159, s[74:75]\n"
      "s_waitcnt lgkmcnt(2)\n"
      "v_mfma_f32_16x16x32_bf16 v[144:147], v[132:135], v[92:95], v[144:147]\n"
      "v_add_f32 v148, v148, v149\n"
      "v_add_f32 v150, v150, v151\n"
      "v_mfma_f32_16x16x32_bf16 v[160:163], v[132:135], v[124:127], v[160:163]\n"
      "v_add_f32 v152, v152, v153\n"
      "v_add_f32 v154, v154, v155\n"
      "s_waitcnt lgkmcnt(0)\n"
      "v_mfma_f32_16x16x32_bf16 v[144:147], v[136:139], v[96:99], v[144:147]\n"
      "v_add_f32 v148, v148, v150\n"
      "v_add_f32 v152, v152, v154\n"
      "v_mfma_f32_16x16x32_bf16 v[160:163], v[136:139], v[128:131], v[160:163]\n"
      "v_add_f32 v148, v148, v152\n"
      "v_mov_b32 v149, v148\n"
      "v_lshl_add_u32 v151, v0, 5, s46\n"
      "v_mov_b32 v150, s49\n"
      "v_permlane16_swap_b32 v148, v149\n"
      "v_add_f32 v148, v148, v149\n"
      "v_mov_b32 v149, v148\n"
      "ds_write2st64_b32 v2, v144, v145 offset0:0 offset1:4\n"
      "ds_write2st64_b32 v2, v146, v147 offset0:1 offset1:5\n"
      "v_permlane32_swap_b32 v148, v149\n"
      "ds_write2st64_b32 v2, v160, v161 offset0:2 offset1:6\n"
      "ds_write2st64_b32 v2, v162, v163 offset0:3 offset1:7\n"
      "v_add_f32 v148, v148, v149\n"
      "s_mov_b64 exec, 3\n"
      "ds_write2_b32 v151, v165, v148 offset0:0 offset1:4\n"
      "s_mov_b64 exec, -1\n"
      "s_waitcnt lgkmcnt(0)\n"
      "s_barrier\n"
      "ds_read_b128 v[132:135], v150\n"
      "ds_read_b128 v[136:139], v150 offset:16\n"
      "ds_read_b64 v[140:141], v3 offset:0\n"
      "ds_read_b64 v[142:143], v3 offset:2048\n"
      "ds_read_b64 v[144:145], v3 offset:4096\n"
      "ds_read_b64 v[146:147], v3 offset:6144\n"
      "s_waitcnt lgkmcnt(5)\n"
      "v_max3_f32 v148, v132, v133, v134\n"
      "v_max_f32 v148, v148, v135\n"
      "v_sub_f32 v132, v132, v148\n"
      "v_sub_f32 v133, v133, v148\n"
      "v_exp_f32 v132, v132\n"
      "v_sub_f32 v134, v134, v148\n"
      "v_exp_f32 v133, v133\n"
      "v_sub_f32 v135, v135, v148\n"
      "v_exp_f32 v134, v134\n"
      "v_exp_f32 v135, v135\n"
      "s_waitcnt lgkmcnt(3)\n"
      "v_pk_mul_f32 v[136:137], v[136:137], v[132:133]\n"
      "v_pk_mul_f32 v[140:141], v[140:141], v[132:133] op_sel_hi:[1,0]\n"
      "v_pk_fma_f32 v[136:137], v[138:139], v[134:135], v[136:137]\n"
      "s_waitcnt lgkmcnt(1)\n"
      "v_pk_fma_f32 v[140:141], v[142:143], v[132:133], v[140:141] op_sel:[0,1,0] op_sel_hi:[1,1,1]\n"
      "v_add_f32 v136, v136, v137\n"
      "v_rcp_f32 v138, v136\n"
      "v_pk_fma_f32 v[140:141], v[144:145], v[134:135], v[140:141] op_sel_hi:[1,0,1]\n"
      "s_waitcnt lgkmcnt(0)\n"
      "v_pk_fma_f32 v[140:141], v[146:147], v[134:135], v[140:141] op_sel:[0,1,0] op_sel_hi:[1,1,1]\n"
      "v_pk_mul_f32 v[140:141], v[140:141], v[138:139] op_sel_hi:[1,0]\n"
      "v_log_f32 v136, v136\n"
      "v_cvt_pk_bf16_f32 v140, v140, v141\n"
      "v_min_u32 v140, -2, v140\n"
      "s_cmp_eq_u32 s44, 0\n"
      "buffer_store_dword v140, v164, s[24:27], s50 offen sc1\n"
      "s_cbranch_scc0 .Lfk_ns109\n"
      "v_add_f32 v136, v136, v148\n"
      "v_mov_b32 v137, s23\n"
      "v_lshlrev_b32 v138, 3, v0\n"
      "s_mov_b64 exec, 0xffff\n"
      "buffer_store_dwordx2 v[136:137], v138, s[24:27], s51 offen sc1\n"
      "s_mov_b64 exec, -1\n"
      ".Lfk_ns109:\n"
      ".Lfkpa0e:\n"
      "s_cbranch_execz .Lfkpf0d\n"
      "s_waitcnt lgkmcnt(0)\n"
      "s_barrier\n"
      "s_cmp_lg_u32 s86, 0\n"
      "s_cbranch_scc1 .Lfk_ab110\n"
      "s_sleep 30\n"
      "s_branch .Lfk_ab111\n"
      ".Lfk_ab110:\n"
      "s_barrier\n"
      ".Lfk_ab111:\n"
      "s_cmp_eq_u32 s94, 2\n"
      "s_cbranch_scc1 .Lfk_ab112\n"
      "s_cmp_gt_u32 s22, s94\n"
      "s_cbranch_scc1 .Lfk_ab112\n"
      "v_mov_b32 v132, -1\n"
      "v_mov_b32 v133, -1\n"
      "v_mov_b32 v134, -1\n"
      "v_mov_b32 v135, -1\n"
      "v_lshlrev_b32 v136, 4, v0\n"
      "s_lshl_b32 s40, s22, 4\n"
      "s_add_u32 s40, s40, s37\n"
      "s_lshl_b32 s40, s40, 10\n"
      "s_add_u32 s40, s40, s38\n"
      "s_add_u32 s40, s40, 32768\n"
      "buffer_store_dwordx4 v[132:135], v136, s[24:27], s40 offen sc1\n"
      "s_waitcnt vmcnt(0)\n"
      ".Lfk_ab112:\n"
      "s_barrier\n"
      "s_barrier\n"
      "s_branch .Lfk_a1113\n"
      ".Lfk_rt108:\n"
      "s_sub_u32 s59, s59, 1\n"
      "s_cbranch_scc1 .Lfk_q0107\n"
      "s_sleep 2\n"
      "s_waitcnt lgkmcnt(0)\n"
      "s_mov_b64 exec, vcc\n"
      "buffer_load_dwordx4 v[140:143], v1, s[24:27], s47 offen sc1\n"
      "buffer_load_dwordx4 v[144:147], v1, s[24:27], s47 offen offset:1024 sc1\n"
      "s_waitcnt vmcnt(0)\n"
      "s_mov_b64 exec, -1\n"
      "s_branch .Lfk_q0106\n"
      ".Lfk_a1113:\n"
      "s_add_u32 s50, s38, 32768\n"
      "s_add_u32 s51, s45, 8192\n"
      "s_branch .Lfkpa1b\n"
      ".p2align 12\n"
      ".Lfkpa1b:\n"
      "s_cbranch_execz .Lfkpret\n"
      "v_mov_b32 v160, 1472\n"
      "ds_read_b32 v161, v160\n"
      "v_and_b32 v160, 3, v0\n"
      "v_lshlrev_b32 v160, 7, v160\n"
      "s_add_u32 s52, s38, 114688\n"
      "s_mov_b32 s59, 65536\n"
      "s_waitcnt lgkmcnt(0)\n"
      "v_readfirstlane_b32 s90, v161\n"
      "s_nop 3\n"
      "s_memrealtime s[92:93]\n"
      "s_add_u32 s91, s90, 161\n"
      "s_waitcnt lgkmcnt(0)\n"
      "s_sub_u32 s92, s92, s91\n"
      "s_cmp_gt_i32 s92, 0\n"
      "s_cselect_b32 s93, 14, 0\n"
      "s_max_i32 s92, s92, 0\n"
      "s_add_u32 s90, s90, s92\n"
      "s_add_u32 s90, s90, s93\n"
      "s_add_u32 s91, s90, 179\n"
      ".Lfk_un114:\n"
      "s_memrealtime s[92:93]\n"
      "s_waitcnt lgkmcnt(0)\n"
      "s_sub_u32 s92, s92, s91\n"
      "s_cmp_gt_u32 s92, 4294965295\n"
      "s_cbranch_scc1 .Lfk_un114\n"
      "buffer_load_dwordx4 v[156:159], v160, s[24:27], s52 offen sc1\n"
      "s_waitcnt lgkmcnt(0)\n"
      "s_barrier\n"
      "ds_read_b128 v[132:135], v1 offset:0\n"
      "ds_read_b128 v[136:139], v1 offset:64\n"
      "ds_read_b128 v[140:143], v1 offset:128\n"
      "ds_read_b128 v[144:147], v1 offset:192\n"
      "s_waitcnt lgkmcnt(3)\n"
      "v_mfma_f32_16x16x32_bf16 v[148:151], v[4:7], v[132:135], 0\n"
      "v_mfma_f32_16x16x32_bf16 v[152:155], v[36:39], v[132:135], 0\n"
      "ds_read_b128 v[132:135], v1 offset:256\n"
      "s_waitcnt lgkmcnt(3)\n"
      "v_mfma_f32_16x16x32_bf16 v[148:151], v[8:11], v[136:139], v[148:151]\n"
      "v_mfma_f32_16x16x32_bf16 v[152:155], v[40:43], v[136:139], v[152:155]\n"
      "ds_read_b128 v[136:139], v1 offset:320\n"
      "s_waitcnt lgkmcnt(3)\n"
      "v_mfma_f32_16x16x32_bf16 v[148:151], v[12:15], v[140:143], v[148:151]\n"
      "v_mfma_f32_16x16x32_bf16 v[152:155], v[44:47], v[140:143], v[152:155]\n"
      "ds_read_b128 v[140:143], v1 offset:384\n"
      "s_waitcnt lgkmcnt(3)\n"
      "v_mfma_f32_16x16x32_bf16 v[148:151], v[16:19], v[144:147], v[148:151]\n"
      "v_mfma_f32_16x16x32_bf16 v[152:155], v[48:51], v[144:147], v[152:155]\n"
      "ds_read_b128 v[144:147], v1 offset:448\n"
      "s_waitcnt lgkmcnt(3)\n"
      "v_mfma_f32_16x16x32_bf16 v[148:151], v[20:23], v[132:135], v[148:151]\n"
      "v_mfma_f32_16x16x32_bf16 v[152:155], v[52:55], v[132:135], v[152:155]\n"
      ".Lfk_st115:\n"
      "s_waitcnt vmcnt(0)\n"
      "v_cmp_ne_u32 vcc, s23, v157\n"
      "v_cmp_ne_u32 s[56:57], s23, v159\n"
      "v_add_f32_dpp v161, v156, v156 quad_perm:[1,0,3,2] row_mask:0xf bank_mask:0xf\n"
      "v_add_f32_dpp v162, v158, v158 quad_perm:[1,0,3,2] row_mask:0xf bank_mask:0xf\n"
      "s_or_b64 vcc, vcc, s[56:57]\n"
      "s_cbranch_vccnz .Lfk_rt117\n"
      ".Lfk_st116:\n"
      "s_waitcnt lgkmcnt(2)\n"
      "v_mfma_f32_16x16x32_bf16 v[148:151], v[24:27], v[136:139], v[148:151]\n"
      "v_mfma_f32_16x16x32_bf16 v[152:155], v[56:59], v[136:139], v[152:155]\n"
      "v_add_f32_dpp v161, v161, v161 quad_perm:[2,3,0,1] row_mask:0xf bank_mask:0xf\n"
      "v_add_f32_dpp v162, v162, v162 quad_perm:[2,3,0,1] row_mask:0xf bank_mask:0xf\n"
      "v_mul_f32 v161, 0x3b124925, v161\n"
      "v_mul_f32 v162, 0x3b124925, v162\n"
      "s_waitcnt lgkmcnt(1)\n"
      "v_mfma_f32_16x16x32_bf16 v[148:151], v[28:31], v[140:143], v[148:151]\n"
      "v_mfma_f32_16x16x32_bf16 v[152:155], v[60:63], v[140:143], v[152:155]\n"
      "v_fma_f32 v162, -v161, v161, v162\n"
      "v_max_f32 v162, 0, v162\n"
      "v_add_f32 v162, 0x3727c5ac, v162\n"
      "v_rsq_f32 v162, v162\n"
      "s_waitcnt lgkmcnt(0)\n"
      "v_mfma_f32_16x16x32_bf16 v[148:151], v[32:35], v[144:147], v[148:151]\n"
      "v_mfma_f32_16x16x32_bf16 v[152:155], v[64:67], v[144:147], v[152:155]\n"
      "v_mul_f32 v162, s81, v162\n"
      "s_nop 5\n"
      "v_max3_f32 v132, v148, v149, v150\n"
      "v_max3_f32 v133, v151, v152, v153\n"
      "v_max3_f32 v132, v132, v154, v155\n"
      "v_max_f32 v132, v132, v133\n"
      "v_mov_b32 v133, v132\n"
      "s_nop 1\n"
      "v_permlane16_swap_b32 v132, v133\n"
      "v_max_f32 v132, v132, v133\n"
      "v_mov_b32 v133, v132\n"
      "s_nop 1\n"
      "v_permlane32_swap_b32 v132, v133\n"
      "v_max_f32 v132, v132, v133\n"
      "v_mul_f32 v165, v162, v132\n"
      "v_fma_f32 v148, v148, v162, -v165\n"
      "v_fma_f32 v149, v149, v162, -v165\n"
      "v_exp_f32 v148, v148\n"
      "v_fma_f32 v150, v150, v162, -v165\n"
      "v_exp_f32 v149, v149\n"
      "v_fma_f32 v151, v151, v162, -v165\n"
      "v_exp_f32 v150, v150\n"
      "v_cvt_pk_bf16_f32 v156, v148, v149\n"
      "v_fma_f32 v152, v152, v162, -v165\n"
      "v_exp_f32 v151, v151\n"
      "v_fma_f32 v153, v153, v162, -v165\n"
      "v_exp_f32 v152, v152\n"
      "v_cvt_pk_bf16_f32 v157, v150, v151\n"
      "v_fma_f32 v154, v154, v162, -v165\n"
      "v_exp_f32 v153, v153\n"
      "v_fma_f32 v155, v155, v162, -v165\n"
      "v_exp_f32 v154, v154\n"
      "v_cvt_pk_bf16_f32 v158, v152, v153\n"
      "v_exp_f32 v155, v155\n"
      "v_cndmask_b32 v132, 0, v156, s[60:61]\n"
      "v_cndmask_b32 v133, 0, v157, s[60:61]\n"
      "v_cndmask_b32 v134, 0, v158, s[60:61]\n"
      "v_cvt_pk_bf16_f32 v159, v154, v155\n"
      "v_cndmask_b32 v136, 0, v156, s[62:63]\n"
      "v_cndmask_b32 v137, 0, v157, s[62:63]\n"
      "v_cndmask_b32 v138, 0, v158, s[62:63]\n"
      "v_cndmask_b32 v135, 0, v159, s[60:61]\n"
      "v_cndmask_b32 v139, 0, v159, s[62:63]\n"
      "s_nop 0\n"
      "v_mfma_f32_16x16x32_bf16 v[144:147], v[132:135], v[68:71], 0\n"
      "v_cndmask_b32 v140, 0, v156, s[64:65]\n"
      "v_cndmask_b32 v141, 0, v157, s[64:65]\n"
      "v_mfma_f32_16x16x32_bf16 v[160:163], v[132:135], v[100:103], 0\n"
      "v_cndmask_b32 v142, 0, v158, s[64:65]\n"
      "v_cndmask_b32 v143, 0, v159, s[64:65]\n"
      "v_mfma_f32_16x16x32_bf16 v[144:147], v[136:139], v[72:75], v[144:147]\n"
      "v_cndmask_b32 v132, 0, v156, s[66:67]\n"
      "v_cndmask_b32 v133, 0, v157, s[66:67]\n"
      "v_mfma_f32_16x16x32_bf16 v[160:163], v[136:139], v[104:107], v[160:163]\n"
      "v_cndmask_b32 v134, 0, v158, s[66:67]\n"
      "v_cndmask_b32 v135, 0, v159, s[66:67]\n"
      "v_mfma_f32_16x16x32_bf16 v[144:147], v[140:143], v[76:79], v[144:147]\n"
      "v_cndmask_b32 v136, 0, v156, s[68:69]\n"
      "v_cndmask_b32 v137, 0, v157, s[68:69]\n"
      "v_mfma_f32_16x16x32_bf16 v[160:163], v[140:143], v[108:111], v[160:163]\n"
      "v_cndmask_b32 v138, 0, v158, s[68:69]\n"
      "v_cndmask_b32 v139, 0, v159, s[68:69]\n"
      "v_mfma_f32_16x16x32_bf16 v[144:147], v[132:135], v[80:83], v[144:147]\n"
      "v_cndmask_b32 v140, 0, v156, s[70:71]\n"
      "v_cndmask_b32 v141, 0, v157, s[70:71]\n"
      "v_mfma_f32_16x16x32_bf16 v[160:163], v[132:135], v[112:115], v[160:163]\n"
      "v_cndmask_b32 v142, 0, v158, s[70:71]\n"
      "v_cndmask_b32 v143, 0, v159, s[70:71]\n"
      ".Lfkpa1d:\n"
      "s_cbranch_execz .Lfkpret\n"
      "v_mfma_f32_16x16x32_bf16 v[144:147], v[136:139], v[84:87], v[144:147]\n"
      "v_cndmask_b32 v132, 0, v156, s[72:73]\n"
      "v_cndmask_b32 v133, 0, v157, s[72:73]\n"
      "v_mfma_f32_16x16x32_bf16 v[160:163], v[136:139], v[116:119], v[160:163]\n"
      "v_cndmask_b32 v134, 0, v158, s[72:73]\n"
      "v_cndmask_b32 v135, 0, v159, s[72:73]\n"
      "v_mfma_f32_16x16x32_bf16 v[144:147], v[140:143], v[88:91], v[144:147]\n"
      "v_cndmask_b32 v136, 0, v156, s[74:75]\n"
      "v_cndmask_b32 v137, 0, v157, s[74:75]\n"
      "v_mfma_f32_16x16x32_bf16 v[160:163], v[140:143], v[120:123], v[160:163]\n"
      "v_cndmask_b32 v138, 0, v158, s[74:75]\n"
      "v_cndmask_b32 v139, 0, v159, s[74:75]\n"
      "v_mfma_f32_16x16x32_bf16 v[144:147], v[132:135], v[92:95], v[144:147]\n"
      "v_add_f32 v148, v148, v149\n"
      "v_add_f32 v150, v150, v151\n"
      "v_mfma_f32_16x16x32_bf16 v[160:163], v[132:135], v[124:127], v[160:163]\n"
      "v_add_f32 v152, v152, v153\n"
      "v_add_f32 v154, v154, v155\n"
      "v_mfma_f32_16x16x32_bf16 v[144:147], v[136:139], v[96:99], v[144:147]\n"
      "v_add_f32 v148, v148, v150\n"
      "v_add_f32 v152, v152, v154\n"
      "v_mfma_f32_16x16x32_bf16 v[160:163], v[136:139], v[128:131], v[160:163]\n"
      "v_add_f32 v148, v148, v152\n"
      "v_mov_b32 v149, v148\n"
      "v_lshl_add_u32 v151, v0, 5, s46\n"
      "v_mov_b32 v150, s49\n"
      "v_permlane16_swap_b32 v148, v149\n"
      "v_add_f32 v148, v148, v149\n"
      "v_mov_b32 v149, v148\n"
      "ds_write2st64_b32 v2, v144, v145 offset0:0 offset1:4\n"
      "ds_write2st64_b32 v2, v146, v147 offset0:1 offset1:5\n"
      "v_permlane32_swap_b32 v148, v149\n"
      "ds_write2st64_b32 v2, v160, v161 offset0:2 offset1:6\n"
      "ds_write2st64_b32 v2, v162, v163 offset0:3 offset1:7\n"
      "v_add_f32 v148, v148, v149\n"
      "s_mov_b64 exec, 3\n"
      "ds_write2_b32 v151, v165, v148 offset0:0 offset1:4\n"
      "s_mov_b64 exec, -1\n"
      "s_waitcnt lgkmcnt(0)\n"
      "s_barrier\n"
      "ds_read_b128 v[132:135], v150\n"
      "ds_read_b128 v[136:139], v150 offset:16\n"
      "ds_read_b64 v[140:141], v3 offset:0\n"
      "ds_read_b64 v[142:143], v3 offset:2048\n"
      "ds_read_b64 v[144:145], v3 offset:4096\n"
      "ds_read_b64 v[146:147], v3 offset:6144\n"
      "s_waitcnt lgkmcnt(5)\n"
      "v_max3_f32 v148, v132, v133, v134\n"
      "v_max_f32 v148, v148, v135\n"
      "v_sub_f32 v132, v132, v148\n"
      "v_sub_f32 v133, v133, v148\n"
      "v_exp_f32 v132, v132\n"
      "v_sub_f32 v134, v134, v148\n"
      "v_exp_f32 v133, v133\n"
      "v_sub_f32 v135, v135, v148\n"
      "v_exp_f32 v134, v134\n"
      "v_exp_f32 v135, v135\n"
      "s_waitcnt lgkmcnt(3)\n"
      "v_pk_mul_f32 v[136:137], v[136:137], v[132:133]\n"
      "v_pk_mul_f32 v[140:141], v[140:141], v[132:133] op_sel_hi:[1,0]\n"
      "v_pk_fma_f32 v[136:137], v[138:139], v[134:135], v[136:137]\n"
      "s_waitcnt lgkmcnt(1)\n"
      "v_pk_fma_f32 v[140:141], v[142:143], v[132:133], v[140:141] op_sel:[0,1,0] op_sel_hi:[1,1,1]\n"
      "v_add_f32 v136, v136, v137\n"
      "v_rcp_f32 v138, v136\n"
      "v_pk_fma_f32 v[140:141], v[144:145], v[134:135], v[140:141] op_sel_hi:[1,0,1]\n"
      "s_waitcnt lgkmcnt(0)\n"
      "v_pk_fma_f32 v[140:141], v[146:147], v[134:135], v[140:141] op_sel:[0,1,0] op_sel_hi:[1,1,1]\n"
      "v_pk_mul_f32 v[140:141], v[140:141], v[138:139] op_sel_hi:[1,0]\n"
      "v_log_f32 v136, v136\n"
      "v_cvt_pk_bf16_f32 v140, v140, v141\n"
      "v_min_u32 v140, -2, v140\n"
      "s_cmp_eq_u32 s44, 0\n"
      "buffer_store_dword v140, v164, s[24:27], s50 offen sc1\n"
      "s_cbranch_scc0 .Lfk_ns118\n"
      "v_add_f32 v136, v136, v148\n"
      "v_mov_b32 v137, s23\n"
      "v_lshlrev_b32 v138, 3, v0\n"
      "s_mov_b64 exec, 0xffff\n"
      "buffer_store_dwordx2 v[136:137], v138, s[24:27], s51 offen sc1\n"
      "s_mov_b64 exec, -1\n"
      ".Lfk_ns118:\n"
      "s_endpgm\n"
      ".Lfk_rt117:\n"
      "s_sub_u32 s59, s59, 1\n"
      "s_cbranch_scc1 .Lfk_st116\n"
      "s_sleep 2\n"
      "s_waitcnt lgkmcnt(0)\n"
      "s_mov_b64 exec, vcc\n"
      "buffer_load_dwordx4 v[156:159], v160, s[24:27], s52 offen sc1\n"
      "s_mov_b64 exec, -1\n"
      "s_branch .Lfk_st115\n"
      :
      : "{s[0:1]}"(__builtin_amdgcn_kernarg_segment_ptr()), "{s2}"(__builtin_amdgcn_workgroup_id_x()), "{v0}"(__builtin_amdgcn_workitem_id_x())
      : "v1", "v2", "v3", "v4", "v5", "v6", "v7", "v8", "v9", "v10", "v11", "v12", "v13", "v14", "v15", "v16", "v17", "v18", "v19", "v20", "v21", "v22", "v23", "v24", "v25", "v26", "v27", "v28", "v29", "v30", "v31", "v32", "v33", "v34", "v35", "v36", "v37", "v38", "v39", "v40", "v41", "v42", "v43", "v44", "v45", "v46", "v47", "v48", "v49", "v50", "v51", "v52", "v53", "v54", "v55", "v56", "v57", "v58", "v59", "v60", "v61", "v62", "v63", "v64", "v65", "v66", "v67", "v68", "v69", "v70", "v71", "v72", "v73", "v74", "v75", "v76", "v77", "v78", "v79", "v80", "v81", "v82", "v83", "v84", "v85", "v86", "v87", "v88", "v89", "v90", "v91", "v92", "v93", "v94", "v95", "v96", "v97", "v98", "v99", "v100", "v101", "v102", "v103", "v104", "v105", "v106", "v107", "v108", "v109", "v110", "v111", "v112", "v113", "v114", "v115", "v116", "v117", "v118", "v119", "v120", "v121", "v122", "v123", "v124", "v125", "v126", "v127", "v128", "v129", "v130", "v131", "v132", "v133", "v134", "v135", "v136", "v137", "v138", "v139", "v140", "v141", "v142", "v143", "v144", "v145", "v146", "v147", "v148", "v149", "v150", "v151", "v152", "v153", "v154", "v155", "v156", "v157", "v158", "v159", "v160", "v161", "v162", "v163", "v164", "v165", "v166", "v167", "s3", "s4", "s5", "s6", "s7", "s8", "s9", "s10", "s11", "s12", "s13", "s14", "s15", "s16", "s17", "s18", "s19", "s20", "s21", "s22", "s23", "s24", "s25", "s26", "s27", "s28", "s29", "s30", "s31", "s32", "s33", "s34", "s35", "s36", "s37", "s38", "s39", "s40", "s41", "s42", "s43", "s44", "s45", "s46", "s47", "s48", "s49", "s50", "s51", "s52", "s53", "s54", "s55", "s56", "s57", "s58", "s59", "s60", "s61", "s62", "s63", "s64", "s65", "s66", "s67", "s68", "s69", "s70", "s71", "s72", "s73", "s74", "s75", "s76", "s77", "s78", "s79", "s80", "s81", "s82", "s83", "s84", "s85", "s86", "s87", "s88", "s89", "s90", "s91", "s92", "s93", "s94", "s95", "s96", "s97", "s98", "s99", "vcc", "scc", "memory");
#endif
}
}

extern "C" void kernel_launch(void* const* d_in, const int* in_sizes, int n_in, void* d_out, int out_size, void* d_ws, size_t ws_size,
                              hipStream_t stream) {
  (void)hipFuncSetAttribute((const void*)fused_kernel, hipFuncAttributeMaxDynamicSharedMemorySize, 163840);
  fused_kernel<<<256, 768, 163840, stream>>>(d_in[0], d_in[1], d_in[2], d_in[3], d_in[4], d_in[6], d_in[7], d_out, d_ws);
}
```
